```python
import torch

def _softmax(x, axis=-1):
    return torch.softmax(x, dim=axis)
def _gelu(x):
    return torch.nn.functional.gelu(x, approximate="tanh")
def _silu(x):
    return torch.nn.functional.silu(x)
def _sigmoid(x):
    return torch.sigmoid(x)
def _layernorm(x, axis=-1, eps=1e-5):
    m = x.mean(dim=axis, keepdim=True)
    v = x.var(dim=axis, keepdim=True, unbiased=False)
    return (x - m) / torch.sqrt(v + eps)
def _scatter_add(tgt, idx, src, axis=0):
    return tgt.clone().index_add_(axis, idx, src)
def _segment_sum(src, seg, n):
    out = torch.zeros((n,) + src.shape[1:], dtype=src.dtype)
    return out.index_add_(0, seg, src)
def _pad(x, pw):
    flat = tuple(v for pair in reversed(pw) for v in pair)
    return torch.nn.functional.pad(x, flat)

kernel_name = 'parallel_sum__ssm_conv_s4d__glu_gated_swiglu__prompt'

def setup_inputs():

    _w = (lambda a: (lambda t: t.to(torch.bfloat16) if t.is_floating_point() else t)(torch.as_tensor(a)))
    def _A_setup():
        Bsz, L, D, N = 1, 2048, 640, 128
        return {
            "u": _w((lambda *s: torch.randn(*s, dtype=torch.bfloat16) * 0.05)(Bsz, L, D)),
            "log_A_real": _w((lambda *s: torch.randn(*s, dtype=torch.bfloat16) * 3.2)(D, N) * 0.5),
            "A_imag": _w((lambda *s: torch.randn(*s, dtype=torch.bfloat16) * 0.05)(D, N) * 3.14),
            "C_real": _w((lambda *s: torch.randn(*s, dtype=torch.bfloat16) * 0.2)(D, N) / N ** 0.5),
            "C_imag": _w((lambda *s: torch.randn(*s, dtype=torch.bfloat16) * 0.2)(D, N) / N ** 0.5),
            "log_dt": _w((lambda *s: torch.randn(*s, dtype=torch.bfloat16) * 0.05)(D) * 0.5 - 4.0),
            "D_skip": _w((lambda *s: torch.randn(*s, dtype=torch.bfloat16) * 0.05)(D)),
        }
    _A_in = _A_setup()

    def _B_setup():
        B, S, D, F = 1, 2048, 640, 1728
        return {
            "x": _w((lambda *s: torch.randn(*s, dtype=torch.bfloat16) * 0.05)(B, S, D)),
            "w1": _w((lambda *s: torch.randn(*s, dtype=torch.bfloat16) * 0.4)(D, F) * (2.0 / D) ** 0.5),
            "w3": _w((lambda *s: torch.randn(*s, dtype=torch.bfloat16) * 0.4)(D, F) * (2.0 / D) ** 0.5),
            "w2": _w((lambda *s: torch.randn(*s, dtype=torch.bfloat16) * 0.4)(F, D) * (2.0 / F) ** 0.5),
        }
    _B_in = _B_setup()

    return {
        "A_u": _A_in["u"],
        "A_log_A_real": _A_in["log_A_real"],
        "A_A_imag": _A_in["A_imag"],
        "A_C_real": _A_in["C_real"],
        "A_C_imag": _A_in["C_imag"],
        "A_log_dt": _A_in["log_dt"],
        "A_D_skip": _A_in["D_skip"],
        "B_x": _B_in["x"],
        "B_w1": _B_in["w1"],
        "B_w3": _B_in["w3"],
        "B_w2": _B_in["w2"],
    }

def reference(A_u, A_log_A_real, A_A_imag, A_C_real, A_C_imag, A_log_dt, A_D_skip, B_x, B_w1, B_w3, B_w2):
    def _A_ref(u, log_A_real, A_imag, C_real, C_imag, log_dt, D_skip):
        L = u.shape[1]
        dt = torch.exp(log_dt)
        A = -torch.exp(log_A_real) + 1j * A_imag
        C = (C_real + 1j * C_imag) * dt[:, None]
        pos = (lambda x, dt: x.to(dt))(torch.arange(L), log_dt.dtype)
        P = torch.exp((A * dt[:, None])[:, :, None] * pos[None, None, :])
        K = 2.0 * torch.einsum("dn,dnl->dl", C, P).real
        L2 = 2 * L
        u_t = (lambda x, *d: x.permute(*d))(u, 0, 2, 1)
        Kf = (lambda x, *a, **k: torch.fft.rfft(x.float(), *a, **k))(K, L2)
        Uf = (lambda x, *a, **k: torch.fft.rfft(x.float(), *a, **k))(u_t, L2)
        y = (lambda x, *a, **k: torch.fft.irfft(x, *a, **k).to(torch.bfloat16))(Uf * Kf[None, :, :], L2)[:, :, :L]
        y = (lambda x, *d: x.permute(*d))(y, 0, 2, 1)
        return y + u * D_skip[None, None, :]

    def _B_ref(x, w1, w3, w2):
        gate = torch.einsum("bsd,df->bsf", x, w1)
        up = torch.einsum("bsd,df->bsf", x, w3)
        h = _silu(gate) * up
        out = torch.einsum("bsf,fd->bsd", h, w2)
        return out

    x = A_u
    xn = _layernorm(x, axis=-1)
    return x + _A_ref(xn, A_log_A_real, A_A_imag, A_C_real, A_C_imag, A_log_dt, A_D_skip) + _B_ref(xn, B_w1, B_w3, B_w2)
```

```cpp
#include <hip/hip_runtime.h>
#include <hip/hip_bf16.h>
#include <stdint.h>

#define LAS __attribute__((address_space(3)))
#define GAS __attribute__((address_space(1)))
typedef unsigned long long u64;
typedef float f32x2 __attribute__((ext_vector_type(2)));
typedef float f32x4 __attribute__((ext_vector_type(4)));
typedef unsigned u32x2 __attribute__((ext_vector_type(2)));
typedef unsigned u32x4 __attribute__((ext_vector_type(4)));
typedef short s16x4 __attribute__((ext_vector_type(4)));
typedef short s16x8 __attribute__((ext_vector_type(8)));
typedef __bf16 bf16x2 __attribute__((ext_vector_type(2)));
typedef __bf16 bf16x8 __attribute__((ext_vector_type(8)));
typedef _Float16 f16x2 __attribute__((ext_vector_type(2)));
typedef _Float16 f16x8 __attribute__((ext_vector_type(8)));
typedef LAS char* ldsp;
typedef unsigned short u16;

constexpr int L_ = 2048, D_ = 640, NM = 128, F_ = 1728;

constexpr size_t WS_NONCE = 0;
constexpr size_t WS_ARR   = 256;
constexpr size_t WS_LNRDY = WS_ARR + 2048;
constexpr size_t WS_UTRDY = WS_LNRDY + 8 * 128;
constexpr size_t WS_HRDY  = WS_UTRDY + 256;
constexpr size_t WS_CHRDY = WS_HRDY + 8 * 256;
constexpr size_t WS_STATS = 32768;
constexpr size_t WS_UT    = WS_STATS + 2048 * 8;
constexpr size_t WS_T1    = WS_UT + (size_t)D_ * L_ * 2;
constexpr size_t WS_H     = WS_T1 + (size_t)D_ * L_ * 2;
constexpr size_t WS_END   = WS_H + (size_t)L_ * F_ * 2;
static_assert(WS_CHRDY + 640 * 4 * 8 <= WS_STATS, "flags");

__device__ __forceinline__ float bf2f(u16 v) { return __uint_as_float(((unsigned)v) << 16); }
__device__ __forceinline__ float bflo(unsigned v) { return __uint_as_float(v << 16); }
__device__ __forceinline__ float bfhi(unsigned v) { return __uint_as_float(v & 0xffff0000u); }
__device__ __forceinline__ unsigned pk_bf16(float a, float b) { f32x2 v = {a, b}; return __builtin_bit_cast(unsigned, __builtin_convertvector(v, bf16x2)); }
__device__ __forceinline__ unsigned pk_bf16(f32x2 v) { return __builtin_bit_cast(unsigned, __builtin_convertvector(v, bf16x2)); }
__device__ __forceinline__ unsigned pk_f16(float a, float b) { f32x2 v = {a, b}; return __builtin_bit_cast(unsigned, __builtin_convertvector(v, f16x2)); }
__device__ __forceinline__ unsigned pk_f16(f32x2 v) { return __builtin_bit_cast(unsigned, __builtin_convertvector(v, f16x2)); }
__device__ __forceinline__ f32x2 unpk_bf16(unsigned v) { return (f32x2){bflo(v), bfhi(v)}; }
__device__ __forceinline__ float bfr(float a) { return bflo(pk_bf16(a, a)); }
__device__ __forceinline__ void glds16(const void* sbase, unsigned voff, unsigned ldsaddr) {
  asm volatile("s_mov_b32 m0, %2\n\ts_nop 3\n\tglobal_load_lds_dwordx4 %0, %1" ::"v"(voff), "s"(sbase), "s"(ldsaddr) : "memory");
}
__device__ __forceinline__ unsigned ldsa(ldsp p) { return (unsigned)(size_t)p; }
__device__ __forceinline__ ldsp ldsq(unsigned a) { return (ldsp)(size_t)a; }
__device__ __forceinline__ s16x4 trread(unsigned a) { typedef short v4i16_t __attribute__((ext_vector_type(4))); return __builtin_bit_cast(s16x4, __builtin_amdgcn_ds_read_tr16_b64_v4i16((LAS v4i16_t*)(size_t)a)); }
__device__ __forceinline__ s16x8 cat8(s16x4 a, s16x4 b) { return __builtin_shufflevector(a, b, 0, 1, 2, 3, 4, 5, 6, 7); }
#define WAIT_V(n) asm volatile("s_waitcnt vmcnt(" #n ")" ::: "memory")
#define WAIT_L(n) asm volatile("s_waitcnt lgkmcnt(" #n ")" ::: "memory")
#define BAR_L() do { asm volatile("" ::: "memory"); __builtin_amdgcn_s_waitcnt(0xC07F); __builtin_amdgcn_s_barrier(); asm volatile("" ::: "memory"); } while (0)
#define SCHED() __builtin_amdgcn_sched_barrier(0)
#define OPAQUE(x) asm volatile("" : "+v"(x))
__device__ __forceinline__ f32x4 mfma_bf16(s16x8 a, s16x8 b, f32x4 c) { return __builtin_amdgcn_mfma_f32_16x16x32_bf16(__builtin_bit_cast(bf16x8, a), __builtin_bit_cast(bf16x8, b), c, 0, 0, 0); }
__device__ __forceinline__ f32x4 mfma_f16(s16x8 a, s16x8 b, f32x4 c) { return __builtin_amdgcn_mfma_f32_16x16x32_f16(__builtin_bit_cast(f16x8, a), __builtin_bit_cast(f16x8, b), c, 0, 0, 0); }
__device__ __forceinline__ u64 ld_agent(const void* p) { return __hip_atomic_load((const GAS u64*)p, __ATOMIC_RELAXED, __HIP_MEMORY_SCOPE_AGENT); }
__device__ __forceinline__ void st_agent(void* p, u64 v) { __hip_atomic_store((GAS u64*)p, v, __ATOMIC_RELAXED, __HIP_MEMORY_SCOPE_AGENT); }
__device__ __forceinline__ u64 ld_agent_async(const void* sbase) { u64 r; asm volatile("s_nop 4\n\tglobal_load_dwordx2 %0, %1, %2 sc1" : "=v"(r) : "v"(0u), "s"(sbase) : "memory"); return r; }
__device__ __forceinline__ u64 ld_agent_async_v(const void* sbase, unsigned voff) { u64 r; asm volatile("s_nop 4\n\tglobal_load_dwordx2 %0, %1, %2 sc1" : "=v"(r) : "v"(voff), "s"(sbase) : "memory"); return r; }
#define WAIT_V_TIE(n, x) asm volatile("s_waitcnt vmcnt(" #n ")" : "+v"(x)::"memory")
__device__ __forceinline__ void glds16_dev(const void* sbase, unsigned voff, unsigned ldsaddr) {
  asm volatile("s_mov_b32 m0, %2\n\ts_nop 3\n\tglobal_load_lds_dwordx4 %0, %1 sc1" ::"v"(voff), "s"(sbase), "s"(ldsaddr) : "memory");
}
#define BAR_VL(n) do { asm volatile("s_waitcnt vmcnt(" #n ")" ::: "memory"); BAR_L(); } while (0)
constexpr unsigned SPIN_CAP = 1u << 22;
#define TMO(ws, spins, code)

template <int CTRL> __device__ __forceinline__ float dppf(float v) { return __builtin_bit_cast(float, __builtin_amdgcn_update_dpp(0, __builtin_bit_cast(int, v), CTRL, 0xf, 0xf, true)); }
__device__ __forceinline__ float red16(float s) { s += dppf<0x128>(s); s += dppf<0x124>(s); s += dppf<0x122>(s); s += dppf<0x121>(s); return s; }

#define STDECL(n)
#define STR(n, i)
#define STC(n, i)
#define STFLUSH(n, ws, kid, wv)
#define STAMPARG
#define STAMPPASS(n)
#define AST(i) STR(stx, i)
#define ASTC(i) STC(stx, i)
constexpr int KG_REC = 136;
constexpr int KG_TAB = 128 * KG_REC;
constexpr int KC_BYTES = 1296;
constexpr int CV_KSTR = 4160;
constexpr int CV_U = 4 * CV_KSTR;
constexpr int CV_UB = 4608;
constexpr int CV_RED = 24576;

__device__ __forceinline__ f32x2 cmul(f32x2 a, f32x2 b) {
  f32x2 t, r;
  asm("v_pk_mul_f32 %0, %1, %2 op_sel:[0,0] op_sel_hi:[0,1]" : "=v"(t) : "v"(a), "v"(b));
  asm("v_pk_fma_f32 %0, %1, %2, %3 op_sel:[1,1,0] op_sel_hi:[1,0,1] neg_lo:[0,1,0]" : "=v"(r) : "v"(a), "v"(b), "v"(t));
  return r;
}
__device__ __forceinline__ void cmul4(f32x2 (&r)[4], const f32x2 (&a)[4], const f32x2 (&b)[4]) {
  asm("v_pk_mul_f32 %0, %4, %8 op_sel:[0,0] op_sel_hi:[0,1]\n\tv_pk_mul_f32 %1, %5, %9 op_sel:[0,0] op_sel_hi:[0,1]\n\t"
      "v_pk_mul_f32 %2, %6, %10 op_sel:[0,0] op_sel_hi:[0,1]\n\tv_pk_mul_f32 %3, %7, %11 op_sel:[0,0] op_sel_hi:[0,1]\n\t"
      "v_pk_fma_f32 %0, %4, %8, %0 op_sel:[1,1,0] op_sel_hi:[1,0,1] neg_lo:[0,1,0]\n\tv_pk_fma_f32 %1, %5, %9, %1 op_sel:[1,1,0] op_sel_hi:[1,0,1] neg_lo:[0,1,0]\n\t"
      "v_pk_fma_f32 %2, %6, %10, %2 op_sel:[1,1,0] op_sel_hi:[1,0,1] neg_lo:[0,1,0]\n\tv_pk_fma_f32 %3, %7, %11, %3 op_sel:[1,1,0] op_sel_hi:[1,0,1] neg_lo:[0,1,0]"
      : "=&v"(r[0]), "=&v"(r[1]), "=&v"(r[2]), "=&v"(r[3])
      : "v"(a[0]), "v"(a[1]), "v"(a[2]), "v"(a[3]), "v"(b[0]), "v"(b[1]), "v"(b[2]), "v"(b[3]));
}
__device__ __forceinline__ void cmul4h(u32x4& r, const unsigned (&a)[4], const unsigned (&b)[4]) {
  unsigned r0, r1, r2, r3;
  asm("v_pk_mul_f16 %0, %4, %8 op_sel:[0,0] op_sel_hi:[0,1]\n\tv_pk_mul_f16 %1, %5, %9 op_sel:[0,0] op_sel_hi:[0,1]\n\t"
      "v_pk_mul_f16 %2, %6, %10 op_sel:[0,0] op_sel_hi:[0,1]\n\tv_pk_mul_f16 %3, %7, %11 op_sel:[0,0] op_sel_hi:[0,1]\n\t"
      "v_pk_fma_f16 %0, %4, %8, %0 op_sel:[1,1,0] op_sel_hi:[1,0,1] neg_lo:[0,1,0]\n\tv_pk_fma_f16 %1, %5, %9, %1 op_sel:[1,1,0] op_sel_hi:[1,0,1] neg_lo:[0,1,0]\n\t"
      "v_pk_fma_f16 %2, %6, %10, %2 op_sel:[1,1,0] op_sel_hi:[1,0,1] neg_lo:[0,1,0]\n\tv_pk_fma_f16 %3, %7, %11, %3 op_sel:[1,1,0] op_sel_hi:[1,0,1] neg_lo:[0,1,0]\n\ts_nop 1"
      : "=&v"(r0), "=&v"(r1), "=&v"(r2), "=&v"(r3)
      : "v"(a[0]), "v"(a[1]), "v"(a[2]), "v"(a[3]), "v"(b[0]), "v"(b[1]), "v"(b[2]), "v"(b[3]));
  r = (u32x4){r0, r1, r2, r3};
}
__device__ __forceinline__ float exp_acc(float x) {
  const float t = x * 1.44269504f, r = fmaf(x, 1.44269504f, -t) + x * 1.925963e-08f;
  const float e = __builtin_amdgcn_exp2f(t);
  return fmaf(e * 0.69314718f, r, e);
}
__device__ __forceinline__ float rdl(float s, int l) { return __builtin_bit_cast(float, __builtin_amdgcn_readlane(__builtin_bit_cast(int, s), l)); }
__device__ __forceinline__ float wave_sum(float s) { s = red16(s); return (rdl(s, 0) + rdl(s, 16)) + (rdl(s, 32) + rdl(s, 48)); }
template <int CTRL> __device__ __forceinline__ unsigned dppu(unsigned v) { return (unsigned)__builtin_amdgcn_update_dpp(0, (int)v, CTRL, 0xf, 0xf, true); }
struct ConvOrder {
  int tt[36], cc[36], ld[24];
  constexpr ConvOrder() : tt{}, cc{}, ld{} {
    int n = 0, nl = 0; bool sb[8] = {};
    for (int t = 0; t < 8; ++t)
      for (int c = t; c < 8; ++c) {
        if (c == t) { ld[nl++] = 8 + t; ld[nl++] = 16 + t; }
        tt[n] = t; cc[n] = c; ++n;
        if (!sb[c - t]) { sb[c - t] = true; ld[nl++] = c - t; }
      }
  }
};
struct AuxIn { const u16* logA; const u16* Aim; const u16* Cre; const u16* Cim; const u16* logdt; const u16* dskip; };
struct AuxPar { unsigned la, ai, cr, ci, c2r, c2i, ldt, dsk; };
struct AuxCh { float ksc, dsk; };
typedef const __attribute__((address_space(4))) unsigned* cptr32;
__device__ __forceinline__ void aux_load(AuxPar& p, const AuxIn& in, int ch, int v, int lane) {
  const unsigned o2 = (ch * NM + 32 * v + (lane & 31)) * 2, o4 = ch * NM * 2 + lane * 4;
  asm volatile("s_nop 4\n\tglobal_load_ushort %0, %6, %8\n\tglobal_load_ushort %1, %6, %9\n\tglobal_load_ushort %2, %6, %10\n\tglobal_load_ushort %3, %6, %11\n\t"
               "global_load_dword %4, %7, %10\n\tglobal_load_dword %5, %7, %11"
               : "=&v"(p.la), "=&v"(p.ai), "=&v"(p.cr), "=&v"(p.ci), "=&v"(p.c2r), "=&v"(p.c2i)
               : "v"(o2), "v"(o4), "s"(in.logA), "s"(in.Aim), "s"(in.Cre), "s"(in.Cim) : "memory");
  p.ldt = (((cptr32)(size_t)in.logdt)[ch >> 1] >> (16 * (ch & 1))) & 0xffffu;
  p.dsk = (((cptr32)(size_t)in.dskip)[ch >> 1] >> (16 * (ch & 1))) & 0xffffu;
}
#define AUX_WAIT(n, p) asm volatile("s_waitcnt vmcnt(" #n ")" : "+v"((p).la), "+v"((p).ai), "+v"((p).cr), "+v"((p).ci), "+v"((p).c2r), "+v"((p).c2i)::"memory")

template <class PRE>
__device__ __forceinline__ void kgen4(const AuxPar& par, AuxCh& o, unsigned tab, unsigned kc, int v, int lane STAMPARG, PRE pre_barrier) {
  const int i = lane & 15, g = lane >> 4, hf = lane >> 5, t = v * 64 + lane;
  const float dt = bfr(exp_acc(bf2f((u16)par.ldt)));
  unsigned cm = max(max(par.c2r & 0x7fffu, (par.c2r >> 16) & 0x7fffu), max(par.c2i & 0x7fffu, (par.c2i >> 16) & 0x7fffu));
  cm = max(cm, dppu<0x128>(cm)); cm = max(cm, dppu<0x124>(cm)); cm = max(cm, dppu<0x122>(cm)); cm = max(cm, dppu<0x121>(cm));
  cm = max(max((unsigned)__builtin_amdgcn_readlane(cm, 0), (unsigned)__builtin_amdgcn_readlane(cm, 16)), max((unsigned)__builtin_amdgcn_readlane(cm, 32), (unsigned)__builtin_amdgcn_readlane(cm, 48)));
  unsigned ce = cm >> 7; ce = ce < 1u ? 1u : (ce > 230u ? 230u : ce);
  const float s = __uint_as_float((253u - ce) << 23);
  o.ksc = dt / (s * 65536.0f); o.dsk = bf2f((u16)par.dsk);
  {
    const float zr = -bfr(exp_acc(bf2f((u16)par.la))) * dt, zi = bf2f((u16)par.ai) * dt;
    const float a2 = zr * 1.44269504088896341f;
    const float rh = zi * 0.15915494f, rl = fmaf(zi, 0.15915494f, -rh) + zi * 6.4206383e-09f;
    const float p = hf ? 64.0f : 1.0f, ps = hf ? -64.0f : 1.0f;
    const float e = __builtin_amdgcn_exp2f(a2 * p), tt = rh * ps, ph = (tt - __builtin_rintf(tt)) + rl * ps;
    const f32x2 b1 = {e * __builtin_amdgcn_cosf(ph), e * __builtin_amdgcn_sinf(ph)};
    const float sc = 2.0f * s; const f32x2 C = {hf ? bf2f((u16)par.cr) * sc : 1.0f, hf ? -bf2f((u16)par.ci) * sc : 0.0f};
    const f32x2 b2 = cmul(b1, b1), b3 = cmul(b2, b1), b4 = cmul(b2, b2);
    const f32x2 b8 = cmul(b4, b4), A1 = cmul(C, b4);
    f32x2 q[4];
    { const f32x2 a[4] = {b8, b8, C, C}, b[4] = {b4, b8, b8, b4}; cmul4(q, a, b); }
    const f32x2 b12 = q[0], b16 = q[1], A2 = q[2];
    { const f32x2 a[4] = {b16, C, C, A1}, b[4] = {b16, b12, b16, b16}; cmul4(q, a, b); }
    const f32x2 b32 = q[0], A3 = q[1], B0 = q[2], B1 = q[3];
    const f32x2 B2 = cmul(A2, b16), B3 = cmul(A3, b16);
    const unsigned rec = tab + (32 * v + (lane & 31)) * KG_REC;
    if (hf == 0) {
      LAS f32x2* r2 = (LAS f32x2*)(size_t)rec;
      r2[0] = C; r2[1] = A1; r2[2] = A2; r2[3] = A3; r2[4] = b1; r2[5] = b2; r2[6] = b3; r2[7] = b4;
      *(LAS u32x2*)(size_t)(rec + 112) = (u32x2){pk_f16(b16), pk_f16(b32)};
    } else {
      LAS u32x2* r2 = (LAS u32x2*)(size_t)(rec + 64);
      r2[0] = (u32x2){0x00003c00u, pk_f16(b1)}; r2[1] = (u32x2){pk_f16(b2), pk_f16(b3)};
      r2[2] = (u32x2){pk_f16(C), pk_f16(B0)}; r2[3] = (u32x2){pk_f16(A1), pk_f16(B1)}; r2[4] = (u32x2){pk_f16(A2), pk_f16(B2)}; r2[5] = (u32x2){pk_f16(A3), pk_f16(B3)};
    }
  }
  AST(1);
  {
    f32x4 acc[2][4];
#pragma unroll
    for (int e = 0; e < 2; ++e)
#pragma unroll
      for (int qv = 0; qv < 4; ++qv) acc[e][qv] = (f32x4){0.f, 0.f, 0.f, 0.f};
    const unsigned rb = tab + (32 * v + g) * KG_REC;
    unsigned ra = rb + 8 * (i >> 2), rtb = rb + 32 + 8 * (i & 3), rub = rb + 64 + 4 * (i & 3), rcc = rb + 80 + 8 * (i >> 2), rw = rb + 112;
    OPAQUE(ra); OPAQUE(rtb); OPAQUE(rub); OPAQUE(rcc); OPAQUE(rw);
    f32x2 ta[2][4], tb[2][4]; unsigned ub[2][4]; u32x2 cc[2][4], ww[2][4];
#pragma unroll
    for (int ks = 0; ks < 2; ++ks)
#pragma unroll
      for (int jm = 0; jm < 4; ++jm) {
        const int o2 = (16 * ks + 4 * jm) * KG_REC;
        ta[ks][jm] = *(const LAS f32x2*)(size_t)(ra + o2); tb[ks][jm] = *(const LAS f32x2*)(size_t)(rtb + o2); ub[ks][jm] = *(const LAS unsigned*)(size_t)(rub + o2);
        cc[ks][jm] = *(const LAS u32x2*)(size_t)(rcc + o2); ww[ks][jm] = *(const LAS u32x2*)(size_t)(rw + o2);
      }
    SCHED();
#pragma unroll
    for (int ks = 0; ks < 2; ++ks) {
      f32x2 y0[4]; unsigned y0h[4], c0[4], c1[4], w16[4], w32[4];
      cmul4(y0, ta[ks], tb[ks]);
#pragma unroll
      for (int jm = 0; jm < 4; ++jm) { y0h[jm] = pk_f16(y0[jm]); c0[jm] = cc[ks][jm][0]; c1[jm] = cc[ks][jm][1]; w16[jm] = ww[ks][jm][0]; w32[jm] = ww[ks][jm][1]; }
      u32x4 X[2], Y[4];
      cmul4h(X[0], c0, ub[ks]); cmul4h(X[1], c1, ub[ks]);
      Y[0] = (u32x4){y0h[0], y0h[1], y0h[2], y0h[3]};
      cmul4h(Y[1], y0h, w16); cmul4h(Y[2], y0h, w32);
      { const unsigned y1[4] = {Y[1][0], Y[1][1], Y[1][2], Y[1][3]}; cmul4h(Y[3], y1, w32); }
#pragma unroll
      for (int qv = 0; qv < 4; ++qv)
#pragma unroll
        for (int e = 0; e < 2; ++e) acc[e][qv] = mfma_f16(__builtin_bit_cast(s16x8, X[e]), __builtin_bit_cast(s16x8, Y[qv]), acc[e][qv]);
      SCHED();
    }
    const unsigned pt = tab + v * 32 * KG_REC;
    const int sh = g == 0 ? 0 : g == 1 ? 1 : 2;
    const unsigned pidx0 = pt + 4 * ((g == 0 ? 0 : g == 1 ? 256 : 384) + (((g == 3 ? 256 : 0) + i + 1) >> sh)), pu0 = 4 * (16 >> sh);
    const unsigned pidx1 = pt + 4 * (512 + 32 * g + ((i + 1) >> 3));
    if (((i + 1) & ((1 << sh) - 1)) == 0) {
#pragma unroll
      for (int qv = 0; qv < 4; ++qv)
#pragma unroll
        for (int r = 0; r < 4; ++r) *(LAS float*)(size_t)(pidx0 + (4 * r + qv) * pu0) = acc[0][qv][r];
    }
    if ((i & 7) == 7) {
#pragma unroll
      for (int qv = 0; qv < 4; ++qv)
#pragma unroll
        for (int r = 0; r < 4; ++r) *(LAS float*)(size_t)(pidx1 + 4 * (8 * r + 2 * qv)) = acc[1][qv][r];
    }
  }
  AST(2);
  pre_barrier();
  BAR_L();
  float pv[3][4];
#pragma unroll
  for (int k = 0; k < 3; ++k) { const int id = t + 256 * k < 641 ? t + 256 * k : 641;
#pragma unroll
    for (int q = 0; q < 4; ++q) pv[k][q] = *(const LAS float*)(size_t)(tab + q * 32 * KG_REC + 4 * id); }
  SCHED();
#pragma unroll
  for (int k = 0; k < 3; ++k) {
    const int id = t + 256 * k;
    float val = (pv[k][0] + pv[k][1]) + (pv[k][2] + pv[k][3]);
    if (k == 0 && v == 0) { const float k0 = wave_sum(bflo(par.c2r) + bfhi(par.c2r)); if (id == 0) val = 2.0f * s * k0; }
    if (id >= 641) val = 0.f;
    if (id <= 641) *(LAS _Float16*)(size_t)(kc + 2 * id) = (_Float16)(val * 64.0f);
  }
  AST(3);
}

__device__ __forceinline__ void conv_issue_u(const char* ws, int ch, unsigned ub, int v, int lane) {
  glds16_dev(ws + WS_UT + (size_t)ch * (L_ * 2) + v * 1024, lane * 16, ub + 512 + v * 1024);
}
__device__ __forceinline__ void conv_expand(unsigned kc, unsigned cv, unsigned ub, int v, int lane) {
  const int t = v * 64 + lane;
  unsigned ga[12];
  const unsigned gb = kc - 2 * 17024;
#pragma unroll
  for (int k = 0; k < 12; k += 2) {
    const int l = 2047 - 8 * t - k;
    const unsigned pk = pk_bf16((float)l, (float)(l - 1));
    ga[k] = gb + 2 * (pk & 0xffffu); ga[k + 1] = gb + 2 * (pk >> 16);
  }
  if (v == 3) {
#pragma unroll
    for (int k = 0; k < 11; ++k) { const int l = 2047 - 8 * t - k; if (l < 128) ga[k] = kc + 2 * (l < 0 ? 641 : l); }
  }
  unsigned e[11];
#pragma unroll
  for (int k = 0; k < 11; ++k) e[k] = *(const LAS u16*)(size_t)ga[k];
  if (t < 32) *(LAS u32x4*)(size_t)(ub + t * 16) = (u32x4){0u, 0u, 0u, 0u};
  else if (t < 48) *(LAS u32x4*)(size_t)(cv + ((t - 32) >> 2) * CV_KSTR + 4096 + (t & 3) * 16) = (u32x4){0u, 0u, 0u, 0u};
  SCHED();
#pragma unroll
  for (int sg = 0; sg < 4; ++sg) {
    u32x4 o;
#pragma unroll
    for (int q = 0; q < 4; ++q) o[q] = e[2 * q + 3 - sg] | (e[2 * q + 4 - sg] << 16);
    *(LAS u32x4*)(size_t)(cv + sg * CV_KSTR + t * 16) = o;
  }
}
__device__ __forceinline__ void conv_mm(f32x4 (&acc)[8], unsigned cv, unsigned ub, unsigned red, int v, int lane) {
  const int i = lane & 15, g = lane >> 4;
  unsigned pal = cv + (i & 3) * CV_KSTR + (2028 - 4 * (i >> 2) + 8 * g) * 2 - 3648 - 2 * v * 64, pah = pal + 8;
  unsigned pb = ub + 512 + (16 * i - 16 + 8 * g) * 2 - 64 - 2 * v * 64;
  OPAQUE(pal); OPAQUE(pah); OPAQUE(pb);
  s16x4 AL[2][8], AH[2][8]; s16x8 B[2][8];
  constexpr ConvOrder O{};
  auto ld1 = [&](int set, int code) {
    const int k = code & 7;
    if (code < 8) B[set][k] = *(const LAS s16x8*)(size_t)(pb + 64 + k * 512 - set * 64);
    else if (code < 16) AL[set][k] = *(const LAS s16x4*)(size_t)(pal + 3648 - k * 512 - set * 64);
    else AH[set][k] = *(const LAS s16x4*)(size_t)(pah + 3648 - k * 512 - set * 64);
  };
#pragma unroll
  for (int c = 0; c < 8; ++c) acc[c] = (f32x4){0.f, 0.f, 0.f, 0.f};
#pragma unroll
  for (int r = 0; r < 24; ++r) ld1(0, O.ld[r]);
  SCHED();
#pragma unroll
  for (int set = 0; set < 2; ++set)
#pragma unroll
    for (int n = 0; n < 36; ++n) {
      acc[O.cc[n]] = mfma_f16(cat8(AL[set][O.tt[n]], AH[set][O.tt[n]]), B[set][O.cc[n] - O.tt[n]], acc[O.cc[n]]);
      if (set == 0 && n < 24) ld1(1, O.ld[n]);
      if (set == 0) SCHED();
    }
#pragma unroll
  for (int c = 0; c < 8; ++c) if ((c >> 1) != v) *(LAS f32x4*)(size_t)(red + ((((c >> 1) * 3 + ((v - (c >> 1) - 1) & 3)) * 2 + (c & 1)) << 10) + lane * 16) = acc[c];
}
__device__ __forceinline__ void conv_finish(const f32x4 (&acc)[8], const AuxCh& o, char* ws, int ch, bool store, unsigned ub, unsigned red, int v, int lane) {
  const int i = lane & 15, g = lane >> 4;
  f32x4 ot[3][2]; u32x2 uq[2]; int ll[2];
#pragma unroll
  for (int cc = 0; cc < 2; ++cc) {
    ll[cc] = 256 * (2 * v + cc) + 16 * i + 4 * g; uq[cc] = *(const LAS u32x2*)(size_t)(ub + 512 + 2 * ll[cc]);
#pragma unroll
    for (int si = 0; si < 3; ++si) ot[si][cc] = *(const LAS f32x4*)(size_t)(red + (((v * 3 + si) * 2 + cc) << 10) + lane * 16);
  }
  SCHED();
  const auto rs = __builtin_amdgcn_make_buffer_rsrc((u16*)(ws + WS_T1) + (size_t)ch * L_, 0, store ? L_ * 2 : 0, 0x00020000);
#pragma unroll
  for (int cc = 0; cc < 2; ++cc) {
    const f32x4 own = v == 0 ? acc[cc] : v == 1 ? acc[2 + cc] : v == 2 ? acc[4 + cc] : acc[6 + cc];
    const f32x4 y = (own + ot[0][cc]) + (ot[1][cc] + ot[2][cc]);
    const unsigned ua = uq[cc][0], ub2 = uq[cc][1];
    const f16x2 u0 = __builtin_bit_cast(f16x2, ua), u1 = __builtin_bit_cast(f16x2, ub2);
    const f32x2 x0 = (f32x2){(float)u0.x, (float)u0.y} * (1.0f / 1024.0f), x1 = (f32x2){(float)u1.x, (float)u1.y} * (1.0f / 1024.0f);
    const f32x2 y0 = unpk_bf16(pk_bf16((f32x2){y[0], y[1]} * o.ksc)), y1 = unpk_bf16(pk_bf16((f32x2){y[2], y[3]} * o.ksc));
    const f32x2 s0 = unpk_bf16(pk_bf16(x0 * o.dsk)), s1 = unpk_bf16(pk_bf16(x1 * o.dsk));
    const u32x2 ov = {pk_bf16(y0 + s0), pk_bf16(y1 + s1)};
    __builtin_amdgcn_raw_buffer_store_b64(ov, rs, 2 * ll[cc], 0, 16);
  }
}
__device__ __forceinline__ void conv_publish(char* ws, u64 tag, int ch, bool store, int v, int lane) {
  if (store && lane == 0) st_agent(ws + WS_CHRDY + (size_t)(ch * 4 + v) * 8, tag);
}
__device__ __forceinline__ float dot2_ones(unsigned v, float acc) { return __builtin_amdgcn_fdot2_f32_bf16(__builtin_bit_cast(bf16x2, v), __builtin_bit_cast(bf16x2, 0x3f803f80u), acc, false); }
constexpr int LN_STR = 1312;
__device__ __forceinline__ u64 ln_block(const u16* __restrict__ x, char* ws, int blk, int b, ldsp lds STAMPARG) {
  const int tid = threadIdx.x, w = tid >> 6, lane = tid & 63, rq = lane >> 4, q = lane & 15;
  const int tok0 = blk * 64;
  u32x4 v[2][5];
#pragma unroll
  for (int p = 0; p < 2; ++p)
#pragma unroll
    for (int i = 0; i < 5; ++i) v[p][i] = *(const u32x4*)(x + (size_t)(tok0 + w * 8 + p * 4 + rq) * D_ + (q + 16 * i) * 8);
  const u64 tag = ld_agent(ws + WS_NONCE) + 1;
  if (tid == 0) st_agent(ws + WS_ARR + b * 8, tag);
  AST(0);
  float mb[2], rinv[2];
#pragma unroll
  for (int p = 0; p < 2; ++p) {
    float s0 = 0.f, s1 = 0.f;
#pragma unroll
    for (int i = 0; i < 5; ++i)
#pragma unroll
      for (int j = 0; j < 4; j += 2) { s0 = dot2_ones(v[p][i][j], s0); s1 = dot2_ones(v[p][i][j + 1], s1); }
    const float mean = red16(s0 + s1) * (1.0f / 640.0f);
    f32x2 q2 = {0.f, 0.f};
#pragma unroll
    for (int i = 0; i < 5; ++i)
#pragma unroll
      for (int j = 0; j < 4; ++j) { const f32x2 a = unpk_bf16(v[p][i][j]) - mean; q2 = __builtin_elementwise_fma(a, a, q2); }
    const float var = red16(q2.x + q2.y) * (1.0f / 640.0f);
    mb[p] = bfr(mean); rinv[p] = __builtin_amdgcn_rcpf(bfr(__builtin_amdgcn_sqrtf(bfr(bfr(var) + 1e-5f))));
    if (q == 0) st_agent(ws + WS_STATS + (size_t)(tok0 + w * 8 + p * 4 + rq) * 8, ((u64)__float_as_uint(rinv[p]) << 32) | __float_as_uint(mb[p]));
  }
  AST(1);
#pragma unroll
  for (int p = 0; p < 2; ++p) {
    const int lrow = w * 8 + p * 4 + rq;
    const float r1024 = rinv[p] * 1024.0f;
#pragma unroll
    for (int i = 0; i < 5; ++i) {
      u32x4 o;
#pragma unroll
      for (int j = 0; j < 4; ++j) {
        const f32x2 a = unpk_bf16(pk_bf16(unpk_bf16(v[p][i][j]) - mb[p]));
        o[j] = pk_f16(unpk_bf16(pk_bf16(a * r1024)));
      }
      *(LAS u32x4*)(lds + lrow * LN_STR + (q + 16 * i) * 16) = o;
    }
  }
  AST(3);
  BAR_L();
  AST(4);
  const auto rs = __builtin_amdgcn_make_buffer_rsrc((void*)(ws + WS_UT), 0, D_ * L_ * 2, 0x00020000);
  const int g = lane >> 4, qq = (lane & 15) >> 2, pp = lane & 3, ci = lane & 15;
  const unsigned t2 = ldsa(lds) + 64 * LN_STR + w * 2048;
#pragma unroll
  for (int k = 0; k < 5; ++k) {
    const int cb = w + 8 * k;
#pragma unroll
    for (int th = 0; th < 2; ++th) {
      const unsigned a = ldsa(lds) + (th * 32 + 8 * g + qq) * LN_STR + cb * 32 + pp * 8;
      const s16x4 lo = trread(a), hi = trread(a + 4 * LN_STR);
      *(LAS s16x8*)(size_t)(t2 + ci * 128 + (((4 * th + g) ^ (ci & 7)) << 4)) = cat8(lo, hi);
    }
#pragma unroll
    for (int hh = 0; hh < 2; ++hh) {
      const int cr = 8 * hh + (lane >> 3), o = lane & 7;
      const u32x4 v4 = *(const LAS u32x4*)(size_t)(t2 + cr * 128 + ((o ^ (cr & 7)) << 4));
      __builtin_amdgcn_raw_buffer_store_b128(v4, rs, ((cb * 16 + cr) * L_ + tok0 + 8 * o) * 2, 0, 16);
    }
  }
  AST(5);
  BAR_VL(0);
  AST(6);
  if (tid == 0) st_agent(ws + WS_LNRDY + (blk >> 2) * 128 + (blk & 3) * 8, tag);
  if (tid == 64) st_agent(ws + WS_UTRDY + blk * 8, tag);
  return tag;
}

constexpr int G1_SLOT = 49152, G1_W = 32768, G1_CS = 3 * G1_SLOT, KA_LDS = G1_CS + 1024;
struct G1Ctx { const char* xs; const char* wg; const char* wu; unsigned xo[4]; unsigned wo; unsigned ldx; unsigned ldw; };
__device__ __forceinline__ void g1_setup(G1Ctx& c, const u16* x, const u16* w1, const u16* w3, int mt, int nu, ldsp lds, int w, int lane) {
  c.xs = (const char*)(x + (size_t)mt * 256 * D_); c.wg = (const char*)(w1 + nu * 64); c.wu = (const char*)(w3 + nu * 64);
#pragma unroll
  for (int j = 0; j < 4; ++j) { const int row = 32 * w + 8 * j + (lane >> 3); c.xo[j] = row * (D_ * 2) + (((lane & 7) ^ ((row >> 1) & 7)) << 4); }
  { const int row = 8 * w + (lane >> 3), ph = ((row >> 1) & 1) + 2 * ((row >> 3) & 1); c.wo = row * (F_ * 2) + (((lane & 7) ^ (2 * ph)) << 4); }
  c.ldx = ldsa(lds) + w * 4096; c.ldw = ldsa(lds) + G1_W + w * 1024;
}
__device__ __forceinline__ void g1_issue1(const G1Ctx& c, int kt, int slot, int j) {
  if (j < 4) glds16(c.xs + kt * 128, c.xo[j], c.ldx + slot * G1_SLOT + j * 1024);
  else if (j == 4) glds16(c.wg + (size_t)kt * 64 * F_ * 2, c.wo, c.ldw + slot * G1_SLOT);
  else glds16(c.wu + (size_t)kt * 64 * F_ * 2, c.wo, c.ldw + slot * G1_SLOT + 8192);
}
__device__ __forceinline__ void g1_issue(const G1Ctx& c, int kt, int slot) {
#pragma unroll
  for (int j = 0; j < 6; ++j) g1_issue1(c, kt, slot, j);
}
template <class LATE>
__device__ __forceinline__ void g1_main(f32x4 (&acc)[4][4], f32x4& cs, u64 (&st)[4], bool& ut_ok, LATE late, const G1Ctx& c, ldsp lds, int w, int lane, const char* ws, int mt, u64 tag) {
  const int wm = w >> 1, wn = w & 1, fr = lane & 15, g = lane >> 4;
  const int psi = (fr >> 1) & 7;
  const int q = fr >> 2, p = lane & 3, phi = ((q >> 1) & 1) + 2 * (g & 1);
  unsigned xa[3][2], wa[3][4];
#pragma unroll
  for (int sl = 0; sl < 3; ++sl) {
#pragma unroll
    for (int ks = 0; ks < 2; ++ks) { xa[sl][ks] = ldsa(lds) + sl * G1_SLOT + (64 * wm + fr) * 128 + (((4 * ks + g) ^ psi) << 4); OPAQUE(xa[sl][ks]); }
#pragma unroll
    for (int s = 0; s < 4; ++s) { const int b = s ^ wm; const int nb = 2 * wn + (b & 1);
      wa[sl][s] = ldsa(lds) + sl * G1_SLOT + G1_W + (b >> 1) * 8192 + (8 * g + q) * 128 + ((2 * (nb ^ phi) + (p >> 1)) << 4) + 8 * (p & 1); OPAQUE(wa[sl][s]); }
  }
  s16x8 XF[2][4]; s16x4 WL[2][4], WH[2][4];
  const s16x8 ones = {0x3f80, 0x3f80, 0x3f80, 0x3f80, 0x3f80, 0x3f80, 0x3f80, 0x3f80};
  auto rd1 = [&](int set, int slot, int ks, int r) {
    const int code[12] = {0, 1, 8, 2, 3, 4, 5, 6, 7, 9, 10, 11}; r = code[r];
    if (r < 8) { const int s = r >> 1; if (r & 1) WH[set][s] = trread(wa[slot][s] + ks * 4096 + 512); else WL[set][s] = trread(wa[slot][s] + ks * 4096); }
    else XF[set][r - 8] = *(const LAS s16x8*)(size_t)(xa[slot][ks] + (r - 8) * 2048);
  };
  auto mm1 = [&](int set, int i) { const int m = i >> 2, s = i & 3; acc[s][m] = mfma_bf16(cat8(WL[set][s], WH[set][s]), XF[set][m], acc[s][m]); };
  auto half = [&](int set, int rs, int rks, int dkt, int dslot, int d0, int d1) {
#pragma unroll
    for (int j = 0; j < 8; ++j) {
      mm1(set, 2 * j);
      if (rs >= 0 && j < 6) rd1(set ^ 1, rs, rks, 2 * j);
      mm1(set, 2 * j + 1);
      if (rs >= 0 && j < 6) rd1(set ^ 1, rs, rks, 2 * j + 1);
      if (dkt >= 0 && (j & 1) && d0 + (j >> 1) < d1) g1_issue1(c, dkt, dslot, d0 + (j >> 1));
      SCHED();
    }
    cs = mfma_bf16(cat8(WL[set][0], WH[set][0]), ones, cs); SCHED();
  };
#pragma unroll
  for (int s = 0; s < 4; ++s)
#pragma unroll
    for (int m = 0; m < 4; ++m) acc[s][m] = (f32x4){0.f, 0.f, 0.f, 0.f};
  cs = (f32x4){0.f, 0.f, 0.f, 0.f};
  const char* rdy = ws + WS_LNRDY + mt * 128 + (lane & 3) * 8;
  u64 flag = 0, flag2 = 0; bool early = false;
#pragma unroll
  for (int r = 0; r < 12; ++r) rd1(0, 0, 0, r);
  SCHED();
#pragma unroll
  for (int kt = 0; kt < 10; ++kt) {
    half(0, kt % 3, 1, (kt >= 1 && kt + 2 < 10) ? kt + 2 : -1, (kt + 2) % 3, 3, 6);
    if (kt < 9) {
      if (kt < 8) BAR_VL(6); else BAR_VL(0);
      SCHED();
      if (kt == 7) { flag = ld_agent(rdy); flag2 = ld_agent(ws + WS_UTRDY + (lane & 31) * 8); SCHED(); }
      if (kt == 8) {
        early = __all(flag == tag); ut_ok = __all(flag2 == tag);
#pragma unroll
        for (int m = 0; m < 4; ++m) st[m] = ld_agent(ws + WS_STATS + (size_t)(mt * 256 + 64 * wm + 16 * m + fr) * 8);
        late(ut_ok);
        SCHED();
      }
      half(1, (kt + 1) % 3, 0, kt + 3 < 10 ? kt + 3 : -1, kt % 3, 0, 3);
    } else half(1, -1, 0, -1, 0, 0, 0);
  }
  if (!early) {
    unsigned spins = 0;
    while (!__all(ld_agent(rdy) == tag) && ++spins < SPIN_CAP) __builtin_amdgcn_s_sleep(2);
    TMO(ws, spins, 1);
#pragma unroll
    for (int m = 0; m < 4; ++m) st[m] = ld_agent(ws + WS_STATS + (size_t)(mt * 256 + 64 * wm + 16 * m + fr) * 8);
  }
}
template <bool SW>
__device__ __forceinline__ void g1_epi_math(const f32x4 (&acc)[4][4], const LAS float* cst, const u64 (&st)[4], ldsp stg, int w, int lane) {
  const int wm = w >> 1, wn = w & 1, fr = lane & 15, g = lane >> 4;
  if (SW) asm volatile("; swapped epilogue begin"); else asm volatile("; plain epilogue begin");
#pragma unroll
  for (int sp = 0; sp < 2; ++sp) {
    const int j = sp ^ (wm & 1);
    const f32x4 cg = *(const LAS f32x4*)(cst + (wn * 4 + j) * 16 + 4 * g), cu = *(const LAS f32x4*)(cst + (wn * 4 + 2 + j) * 16 + 4 * g);
#pragma unroll
    for (int m = 0; m < 4; ++m) {
      const float mean = __uint_as_float((unsigned)st[m]), rinv = __uint_as_float((unsigned)(st[m] >> 32));
      const f32x4 ga = acc[SW ? sp + 2 : sp][m], ua = acc[SW ? sp : sp + 2][m];
      const float a1 = rinv * -1.44269504088896341f, sd = __builtin_amdgcn_rcpf(rinv), ia2 = sd * sd;
      u32x2 o;
#pragma unroll
      for (int hh = 0; hh < 2; ++hh) {
        const f32x2 nm = {-mean, -mean};
        const f32x2 tg = __builtin_elementwise_fma(nm, (f32x2){cg[2 * hh], cg[2 * hh + 1]}, (f32x2){ga[2 * hh], ga[2 * hh + 1]});
        const f32x2 tu = __builtin_elementwise_fma(nm, (f32x2){cu[2 * hh], cu[2 * hh + 1]}, (f32x2){ua[2 * hh], ua[2 * hh + 1]});
        const f32x2 ea = tg * a1;
        const f32x2 ee = {__builtin_amdgcn_exp2f(ea.x), __builtin_amdgcn_exp2f(ea.y)};
        const f32x2 dn = __builtin_elementwise_fma(ee, (f32x2){ia2, ia2}, (f32x2){ia2, ia2});
        const f32x2 rc = {__builtin_amdgcn_rcpf(dn.x), __builtin_amdgcn_rcpf(dn.y)};
        o[hh] = pk_bf16((tg * tu) * rc);
      }
      const int row = 64 * wm + 16 * m + fr, ch = 4 * wn + 2 * j + (g >> 1);
      *(LAS u32x2*)(stg + row * 128 + ((ch ^ (row & 7)) << 4) + 8 * (g & 1)) = o;
    }
  }
  if (SW) asm volatile("; swapped epilogue end" ::: "memory"); else asm volatile("; plain epilogue end" ::: "memory");
}

constexpr int G2_SLOT = 20480, G2_W = 12288, G2_R = 7;
constexpr int LD_CST = G1_CS;
constexpr int LD_KC = LD_CST + 1024;
constexpr int LD_TAB = 2 * G1_SLOT;
constexpr int LD_U = LD_KC + 2 * KC_BYTES;
constexpr int LD_RED = 40960;
constexpr int LD_CV = 98304;
constexpr int LD_XT = 143360;
constexpr int LD_BYTES = 163840;
static_assert(LD_TAB + 2 * KG_TAB <= G1_CS && LD_U % 16 == 0 && LD_U + 2 * CV_UB <= LD_BYTES && LD_RED + 2 * CV_RED <= LD_CV && LD_CV + 2 * CV_U <= G2_R * G2_SLOT && G2_R * G2_SLOT <= LD_XT && LD_XT + 12288 <= LD_BYTES, "lds map");

struct G2Ctx { const char* hs; const char* wsrc; unsigned ho[3], wo[2], lh, lw; };
struct G2Frag { s16x8 X[2][3]; s16x4 WL[2][2], WH[2][2]; };
__device__ __forceinline__ void g2_setup(G2Ctx& c, const char* ws, const u16* w2, int mt, int nu, unsigned l0, int lw, int lane) {
  c.hs = (const char*)((const u16*)(ws + WS_H) + (size_t)mt * 96 * F_); c.wsrc = (const char*)(w2 + nu * 64);
  c.lh = l0 + lw * 3072; c.lw = l0 + G2_W + lw * 2048;
#pragma unroll
  for (int j = 0; j < 3; ++j) {
    const int pc = 3 * lw + j, row = 8 * pc + (lane >> 3); int r0 = 8 * pc; r0 = mt * 96 + r0 < L_ - 8 ? r0 : L_ - 8 - mt * 96;
    c.ho[j] = (r0 + (lane >> 3)) * (F_ * 2) + (((lane & 7) ^ ((row >> 1) & 7)) << 4);
  }
#pragma unroll
  for (int j = 0; j < 2; ++j) { const int row = 8 * (2 * lw + j) + (lane >> 3), ph = ((row >> 1) & 1) + 2 * ((row >> 3) & 1); c.wo[j] = row * (D_ * 2) + (((lane & 7) ^ (2 * ph)) << 4); }
}
__device__ __forceinline__ void g2_issue_w(const G2Ctx& c, int kt, int slot) {
#pragma unroll
  for (int j = 0; j < 2; ++j) glds16(c.wsrc + (size_t)kt * (64 * D_ * 2), c.wo[j], c.lw + slot * G2_SLOT + j * 1024);
}
__device__ __forceinline__ void g2_issue_h(const G2Ctx& c, int kt, int slot) {
#pragma unroll
  for (int j = 0; j < 3; ++j) glds16_dev(c.hs + kt * 128, c.ho[j], c.lh + slot * G2_SLOT + j * 1024);
}
__device__ __forceinline__ unsigned g2_rdy_off(int mt, int lane) {
  const int a = (mt * 96) >> 8, b = (mt * 96 + 95 < L_ ? mt * 96 + 95 : L_ - 1) >> 8;
  return (unsigned)WS_HRDY + (lane < 32 ? a : b) * 256 + ((lane & 31) < 27 ? (lane & 31) : 0) * 8;
}
__device__ __forceinline__ void gemm2_tile(const u16* __restrict__ x, const u16* __restrict__ w2, char* ws, u16* __restrict__ out, u64 tag, int mt, int nu, G2Ctx& c, bool h01, int pub_ch, ldsp lds, int w, int lane STAMPARG) {
  const unsigned l0 = ldsa(lds);
  const int fr = lane & 15, g = lane >> 4;
  if (w >= 4) {
    const int lw = w - 4;
    if (!h01) {
      const char* rdy = ws + g2_rdy_off(mt, lane);
      unsigned spins = 0;
      while (!__all(ld_agent(rdy) == tag) && ++spins < SPIN_CAP) __builtin_amdgcn_s_sleep(1);
      TMO(ws, spins, 2);
    }
    AST(8);
    if (!h01) { g2_issue_h(c, 0, 0); g2_issue_h(c, 1, 1); }
#pragma unroll
    for (int st = 2; st < G2_R; ++st) { g2_issue_w(c, st, st); g2_issue_h(c, st, st); }
    BAR_VL(25);
    if (pub_ch >= 0) conv_publish(ws, tag, pub_ch, true, w & 3, lane);
    BAR_VL(25);
#pragma unroll
    for (int kt = 1; kt < 21; ++kt) {
      BAR_VL(20);
      g2_issue_w(c, kt + 6, (kt - 1) % G2_R); g2_issue_h(c, kt + 6, (kt - 1) % G2_R);
    }
    BAR_VL(20);
    {
      unsigned xo;
#pragma unroll
      for (int j = 0; j < 3; ++j) { const int pc = 3 * lw + j, row = 8 * pc + (lane >> 3); int r0 = 8 * pc; r0 = mt * 96 + r0 < L_ - 8 ? r0 : L_ - 8 - mt * 96;
        xo = (r0 + (lane >> 3)) * (D_ * 2) + (((lane & 7) ^ (row & 7)) << 4);
        glds16((const char*)(x + (size_t)mt * 96 * D_ + nu * 64), xo, l0 + LD_XT + pc * 1024); }
    }
    BAR_VL(18);
    BAR_VL(13);
    {
      const char* rdy = ws + WS_CHRDY + (size_t)(nu * 64 * 4) * 8 + lane * 8;
      unsigned spins = 0;
      while (!__all(ld_agent(rdy) == tag && ld_agent(rdy + 512) == tag && ld_agent(rdy + 1024) == tag && ld_agent(rdy + 1536) == tag) && ++spins < SPIN_CAP) __builtin_amdgcn_s_sleep(2);
      TMO(ws, spins, 3);
      const char* t1 = ws + WS_T1 + (size_t)nu * 64 * (L_ * 2) + mt * 192;
#pragma unroll
      for (int j = 0; j < 3; ++j) { const int id = (3 * lw + j) * 64 + lane; glds16_dev(t1, (id / 12) * (L_ * 2) + (id % 12) * 16, l0 + (3 * lw + j) * 1024); }
    }
    AST(9);
#pragma unroll
    for (int k = 24; k < 26; ++k) BAR_L();
    BAR_VL(0);
  } else {
    const int wm = w >> 1, wn = w & 1;
    unsigned xa[3][2], wa[3][2];
    {
      const int psi = (fr >> 1) & 7, q = fr >> 2, p = lane & 3, phi = ((q >> 1) & 1) + 2 * (g & 1);
#pragma unroll
      for (int sg = 0; sg < 3; ++sg)
#pragma unroll
        for (int k = 0; k < 2; ++k) {
          xa[sg][k] = l0 + sg * 3 * G2_SLOT + (48 * wm + fr) * 128 + (((4 * k + g) ^ psi) << 4); OPAQUE(xa[sg][k]);
          wa[sg][k] = l0 + sg * 3 * G2_SLOT + G2_W + (8 * g + q) * 128 + ((2 * ((2 * wn + k) ^ phi) + (p >> 1)) << 4) + 8 * (p & 1); OPAQUE(wa[sg][k]);
        }
    }
    f32x4 acc[2][3];
#pragma unroll
    for (int n = 0; n < 2; ++n)
#pragma unroll
      for (int m = 0; m < 3; ++m) acc[n][m] = (f32x4){0.f, 0.f, 0.f, 0.f};
    G2Frag F[2];
    auto rd1 = [&](G2Frag& f, int slot, int r) {
      const int ks = r / 7, q = r % 7, sg = slot / 3, so = (slot % 3) * G2_SLOT;
      if (q == 0) f.WL[ks][0] = trread(wa[sg][0] + so + ks * 4096); else if (q == 1) f.WH[ks][0] = trread(wa[sg][0] + so + ks * 4096 + 512);
      else if (q == 3) f.WL[ks][1] = trread(wa[sg][1] + so + ks * 4096); else if (q == 4) f.WH[ks][1] = trread(wa[sg][1] + so + ks * 4096 + 512);
      else { const int m = q == 2 ? 0 : q - 4; f.X[ks][m] = *(const LAS s16x8*)(size_t)(xa[sg][ks] + so + m * 2048); }
    };
    auto iter = [&](int set, bool rd, int slot) {
#pragma unroll
      for (int n = 0; n < 12; ++n) {
        const int ks = n / 6, m = (n % 6) >> 1, nb = n & 1;
        acc[nb][m] = mfma_bf16(cat8(F[set].WL[ks][nb], F[set].WH[ks][nb]), F[set].X[ks][m], acc[nb][m]);
        if (rd && n < 7) { rd1(F[set ^ 1], slot, 2 * n); rd1(F[set ^ 1], slot, 2 * n + 1); }
        SCHED();
      }
    };
    WAIT_V(0);
    if (pub_ch >= 0) conv_publish(ws, tag, pub_ch, true, w & 3, lane);
    BAR_L();
    AST(13);
#pragma unroll
    for (int r = 0; r < 14; ++r) rd1(F[0], 0, r);
    SCHED();
#pragma unroll
    for (int kt = 0; kt < 26; ++kt) { asm volatile("" ::: "memory"); __builtin_amdgcn_s_barrier(); asm volatile("" ::: "memory"); SCHED(); iter(kt & 1, true, (kt + 1) % G2_R); }
    AST(10);
    iter(0, false, 0);
    BAR_L();
    AST(11);
    s16x4 tq[2][3]; u32x2 xv[2][3]; unsigned xp[2][3];
#pragma unroll
    for (int n = 0; n < 2; ++n)
#pragma unroll
      for (int m = 0; m < 3; ++m) {
        const int s = 2 * wn + n, mb = 3 * wm + m;
        tq[n][m] = trread(l0 + (16 * s + 4 * g + (fr >> 2)) * 192 + (16 * mb + 4 * (lane & 3)) * 2);
        const int row = 16 * mb + fr, ch2 = 2 * s + (g >> 1);
        xp[n][m] = l0 + LD_XT + row * 128 + ((ch2 ^ (row & 7)) << 4) + 8 * (g & 1);
        xv[n][m] = *(const LAS u32x2*)(size_t)xp[n][m];
      }
    SCHED();
#pragma unroll
    for (int n = 0; n < 2; ++n)
#pragma unroll
      for (int m = 0; m < 3; ++m) {
        const f32x4 y = acc[n][m];
        const u32x2 tu = __builtin_bit_cast(u32x2, tq[n][m]);
        const unsigned x0 = xv[n][m][0], x1 = xv[n][m][1], t0 = tu[0], t1 = tu[1];
        const f32x2 a0 = unpk_bf16(pk_bf16(unpk_bf16(x0) + unpk_bf16(t0))), a1 = unpk_bf16(pk_bf16(unpk_bf16(x1) + unpk_bf16(t1)));
        const f32x2 b0 = unpk_bf16(pk_bf16((f32x2){y[0], y[1]})), b1 = unpk_bf16(pk_bf16((f32x2){y[2], y[3]}));
        *(LAS u32x2*)(size_t)xp[n][m] = (u32x2){pk_bf16(a0 + b0), pk_bf16(a1 + b1)};
      }
  }
  BAR_L();
#pragma unroll
  for (int k = 0; k < 2; ++k) {
    const int pc = w + 8 * k;
    if (pc < 12) { const int row = 8 * pc + (lane >> 3), ch2 = lane & 7, tok = mt * 96 + row;
      const u32x4 v = *(const LAS u32x4*)(lds + LD_XT + row * 128 + ((ch2 ^ (row & 7)) << 4));
      if (tok < L_) *(u32x4*)(out + (size_t)tok * D_ + nu * 64 + ch2 * 8) = v; }
  }
}

__global__ __launch_bounds__(512, 2) void k_fused(const u16* __restrict__ x, const u16* __restrict__ w1, const u16* __restrict__ w3, const u16* __restrict__ w2, AuxIn in, char* ws, u16* __restrict__ out) {
  extern __shared__ __attribute__((aligned(16))) char shm[];
  ldsp lds = (ldsp)shm;
  const unsigned l0 = ldsa(lds);
  const int tid = threadIdx.x, w = __builtin_amdgcn_readfirstlane(tid >> 6), lane = tid & 63;
  const int b = blockIdx.x, xcd = b & 7, r = b >> 3, ng = xcd & 3, mg = xcd >> 2;
  const int nn = ng < 3 ? 7 : 6, gq = w >> 2, v = w & 3;
  STDECL(st0); STDECL(st1); STDECL(st2);
  STR(st0, 0);
  const bool gwg = r < 4 * nn;
  int mt2 = -1, nu2 = 0;
  { const int mg2 = xcd >> 1, nm = mg2 < 2 ? 6 : 5, m0 = mg2 < 2 ? 6 * mg2 : 12 + 5 * (mg2 - 2);
    if (r < 5 * nm) { mt2 = m0 + r / 5; nu2 = (xcd & 1) * 5 + r % 5; } }
  u64 tag;
  G2Ctx c2; bool h01 = false; int pub_ch = -1;
  if (gwg) {
    const int mt = mg * 4 + (r & 3), nu = ng * 7 + (r >> 2);
    const int gi = (xcd < 3 ? xcd * 28 : xcd == 3 ? 84 : xcd < 7 ? 108 + (xcd - 4) * 28 : 192) + r;
    const int ch = gi < 210 ? 2 * gi + gq : 420 + (gi - 210);
    const bool chst = gi < 210 || gq == 0;
    G1Ctx c; g1_setup(c, x, w1, w3, mt, nu, lds, w, lane);
    AuxPar par; aux_load(par, in, ch, v, lane);
    u64 nonce = ld_agent_async(ws + WS_NONCE);
    g1_issue(c, 0, 0); g1_issue(c, 1, 1);
    WAIT_V_TIE(12, nonce);
    AUX_WAIT(12, par);
    tag = nonce + 1;
    if (tid == 0) st_agent(ws + WS_ARR + b * 8, tag);
    const unsigned kc = l0 + LD_KC + gq * KC_BYTES, cv = l0 + LD_CV + gq * CV_U, ub = l0 + LD_U + gq * CV_UB, red = l0 + LD_RED + gq * CV_RED;
    AuxCh ach;
    kgen4(par, ach, l0 + LD_TAB + gq * KG_TAB, kc, v, lane STAMPPASS(st1), []() {});
    BAR_VL(0);
    STR(st0, 1); STC(st0, 8);
    g1_issue(c, 2, 2);
    f32x4 acc[4][4], cs; u64 st[4]; bool ut_ok = false;
    g1_main(acc, cs, st, ut_ok, [&](bool ok) { if (ok) conv_issue_u(ws, ch, ub, v, lane); }, c, lds, w, lane, ws, mt, tag);
    STR(st0, 2); STC(st0, 9);
    if (!ut_ok) {
      unsigned spins = 0; while (!__all(ld_agent(ws + WS_UTRDY + (lane & 31) * 8) == tag) && ++spins < SPIN_CAP) __builtin_amdgcn_s_sleep(2); TMO(ws, spins, 4);
      conv_issue_u(ws, ch, ub, v, lane);
    }
    LAS float* cst = (LAS float*)(lds + LD_CST);
    const int wm = w >> 1, wn = w & 1;
    if ((lane & 15) == 0) *(LAS f32x4*)(cst + (wn * 4 + wm) * 16 + 4 * (lane >> 4)) = cs;
    BAR_L();
    ldsp stg = lds + G1_SLOT;
    if (wm & 2) g1_epi_math<true>(acc, cst, st, stg, w, lane); else g1_epi_math<false>(acc, cst, st, stg, w, lane);
    BAR_L();
    STR(st0, 3);
    {
      const auto rs = __builtin_amdgcn_make_buffer_rsrc((void*)(ws + WS_H), 0, L_ * F_ * 2, 0x00020000);
      u32x4 hv[4];
#pragma unroll
      for (int k = 0; k < 4; ++k) { const int row = 32 * w + 8 * k + (lane >> 3), ch2 = lane & 7; hv[k] = *(const LAS u32x4*)(stg + row * 128 + ((ch2 ^ (row & 7)) << 4)); }
#pragma unroll
      for (int k = 0; k < 4; ++k) { const int row = 32 * w + 8 * k + (lane >> 3), ch2 = lane & 7;
        __builtin_amdgcn_raw_buffer_store_b128(hv[k], rs, ((mt * 256 + row) * F_ + nu * 64 + ch2 * 8) * 2, 0, 16); }
    }
    STR(st1, 4);
    conv_expand(kc, cv, ub, v, lane);
    STR(st1, 5);
    BAR_VL(0);
    if (tid == 0) st_agent(ws + WS_HRDY + mt * 256 + nu * 8, tag);
    STR(st0, 4);
    if (mt2 >= 0 && w >= 4) { g2_setup(c2, ws, w2, mt2, nu2, l0, w - 4, lane); g2_issue_w(c2, 0, 0); g2_issue_w(c2, 1, 1); }
    f32x4 cacc[8];
    conv_mm(cacc, cv, ub, red, v, lane);
    STR(st1, 6);
    u64 hf = ld_agent_async_v(ws, g2_rdy_off(mt2 >= 0 ? mt2 : 0, lane));
    BAR_L();
    conv_finish(cacc, ach, ws, ch, chst, ub, red, v, lane);
    WAIT_V_TIE(2, hf);
    if (mt2 >= 0 && w >= 4) {
      h01 = __all(hf == tag);
      if (h01) { g2_issue_h(c2, 0, 0); g2_issue_h(c2, 1, 1); }
    }
    STR(st1, 7);
    pub_ch = chst ? ch : -1;
    BAR_L();
    STR(st0, 5);
  } else {
    const int sid = ng < 3 ? ng * 4 + (r - 28) : 12 + (r - 24);
    const int rounds = mt2 >= 0 ? 2 : 3;
    const int r0 = ng < 3 ? 28 : 24, nwith = (xcd < 3 ? 2 : xcd == 3 ? 6 : xcd < 7 ? 0 : 1), kin = r - r0;
    const int ch0 = 426 + (xcd < 3 ? 20 * xcd : xcd == 3 ? 60 : xcd < 7 ? 96 + 24 * (xcd - 4) : 168) + (kin < nwith ? 4 * kin : 4 * nwith + 6 * (kin - nwith));
    AuxPar par; aux_load(par, in, (ch0 + gq < 640 ? ch0 + gq : 639), v, lane);
    if (sid < 16) tag = ln_block(x, ws, mg * 16 + sid, b, lds STAMPPASS(st2));
    else {
      tag = ld_agent(ws + WS_NONCE) + 1;
      if (tid == 0) st_agent(ws + WS_ARR + b * 8, tag);
      if (sid == 16 && mg == 0 && w == 0) {
        unsigned spins = 0;
        for (;;) {
          bool ok = true;
#pragma unroll
          for (int i = 0; i < 4; ++i) ok &= ld_agent(ws + WS_ARR + (lane + 64 * i) * 8) == tag;
          if (__all(ok) || ++spins >= SPIN_CAP) break;
          __builtin_amdgcn_s_sleep(8);
        }
        TMO(ws, spins, 5);
        if (lane == 0) st_agent(ws + WS_NONCE, tag);
      }
    }
    STR(st0, 6);
    BAR_L();
    const unsigned kc = l0 + LD_KC + gq * KC_BYTES, cv = l0 + LD_CV + gq * CV_U, ub = l0 + LD_U + gq * CV_UB, red = l0 + LD_RED + gq * CV_RED;
#pragma unroll 1
    for (int rd = 0; rd < rounds; ++rd) {
      const int chr = ch0 + 2 * rd + gq; const bool chst = chr < 640; const int ch = chst ? chr : 639;
      AUX_WAIT(0, par);
      if (pub_ch >= 0) { conv_publish(ws, tag, pub_ch, true, v, lane); pub_ch = -1; }
      if (rd == 0) STR(st1, 0);
      AuxCh ach;
      kgen4(par, ach, l0 + gq * KG_TAB, kc, v, lane STAMPPASS(st1), []() {});
      if (rd == 0) {
        unsigned spins = 0;
        while (!__all(ld_agent(ws + WS_UTRDY + (lane & 31) * 8) == tag) && ++spins < SPIN_CAP) __builtin_amdgcn_s_sleep(4);
        TMO(ws, spins, 6);
        STR(st0, 7);
      }
      conv_issue_u(ws, ch, ub, v, lane);
      if (rd + 1 < rounds) { const int cn = ch0 + 2 * (rd + 1) + gq; aux_load(par, in, cn < 640 ? cn : 639, v, lane); }
      BAR_L();
      STR(st1, 4);
      conv_expand(kc, cv, ub, v, lane);
      STR(st1, 5);
      if (rd + 1 < rounds) BAR_VL(6); else BAR_VL(0);
      f32x4 cacc[8];
      conv_mm(cacc, cv, ub, red, v, lane);
      STR(st1, 6);
      BAR_L();
      conv_finish(cacc, ach, ws, ch, chst, ub, red, v, lane);
      STR(st1, 7);
      pub_ch = chst ? ch : -1;
      BAR_L();
      if (rd == 0) { STFLUSH(st1, ws, 3, 0); }
    }
    STR(st0, 5);
    if (mt2 >= 0 && w >= 4) { g2_setup(c2, ws, w2, mt2, nu2, l0, w - 4, lane); g2_issue_w(c2, 0, 0); g2_issue_w(c2, 1, 1); }
  }
  if (mt2 >= 0) gemm2_tile(x, w2, ws, out, tag, mt2, nu2, c2, h01, pub_ch, lds, w, lane STAMPPASS(st0));
  else { WAIT_V(0); if (pub_ch >= 0) conv_publish(ws, tag, pub_ch, true, v, lane); }
  STR(st0, 12);
  STFLUSH(st0, ws, gwg ? 0 : 5, 0); STFLUSH(st0, ws, 1, 4); if (gwg) { STFLUSH(st1, ws, 2, 0); } else { STFLUSH(st2, ws, 4, 0); }
}

extern "C" void kernel_launch(void* const* d_in, const int* in_sizes, int n_in, void* d_out, int out_size, void* d_ws, size_t ws_size, hipStream_t stream) {
  (void)hipFuncSetAttribute((const void*)k_fused, hipFuncAttributeMaxDynamicSharedMemorySize, LD_BYTES);
  AuxIn in{(const u16*)d_in[1], (const u16*)d_in[2], (const u16*)d_in[3], (const u16*)d_in[4], (const u16*)d_in[5], (const u16*)d_in[6]};
  k_fused<<<256, 512, LD_BYTES, stream>>>((const u16*)d_in[0], (const u16*)d_in[8], (const u16*)d_in[9], (const u16*)d_in[10], in, (char*)d_ws, (u16*)d_out);
}
```

```cpp
#include <hip/hip_runtime.h>
#include <hip/hip_bf16.h>
#include <stdint.h>

#define LAS __attribute__((address_space(3)))
#define GAS __attribute__((address_space(1)))
typedef unsigned long long u64;
typedef float f32x2 __attribute__((ext_vector_type(2)));
typedef float f32x4 __attribute__((ext_vector_type(4)));
typedef unsigned u32x2 __attribute__((ext_vector_type(2)));
typedef unsigned u32x4 __attribute__((ext_vector_type(4)));
typedef short s16x4 __attribute__((ext_vector_type(4)));
typedef short s16x8 __attribute__((ext_vector_type(8)));
typedef __bf16 bf16x2 __attribute__((ext_vector_type(2)));
typedef __bf16 bf16x8 __attribute__((ext_vector_type(8)));
typedef _Float16 f16x2 __attribute__((ext_vector_type(2)));
typedef _Float16 f16x8 __attribute__((ext_vector_type(8)));
typedef LAS char* ldsp;
typedef unsigned short u16;

constexpr int L_ = 2048, D_ = 640, NM = 128, F_ = 1728;

constexpr size_t WS_NONCE = 0;
constexpr size_t WS_ARR   = 256;
constexpr size_t WS_LNRDY = WS_ARR + 2048;
constexpr size_t WS_UTRDY = WS_LNRDY + 8 * 128;
constexpr size_t WS_HRDY  = WS_UTRDY + 256;
constexpr size_t WS_CHRDY = WS_HRDY + 8 * 256;
constexpr size_t WS_STATS = 32768;
constexpr size_t WS_UT    = WS_STATS + 2048 * 8;
constexpr size_t WS_T1    = WS_UT + (size_t)D_ * L_ * 2;
constexpr size_t WS_H     = WS_T1 + (size_t)D_ * L_ * 2;
constexpr size_t WS_END   = WS_H + (size_t)L_ * F_ * 2;
static_assert(WS_CHRDY + 640 * 4 * 8 <= WS_STATS, "flags");

__device__ __forceinline__ float bf2f(u16 v) { return __uint_as_float(((unsigned)v) << 16); }
__device__ __forceinline__ float bflo(unsigned v) { return __uint_as_float(v << 16); }
__device__ __forceinline__ float bfhi(unsigned v) { return __uint_as_float(v & 0xffff0000u); }
__device__ __forceinline__ unsigned pk_bf16(float a, float b) { f32x2 v = {a, b}; return __builtin_bit_cast(unsigned, __builtin_convertvector(v, bf16x2)); }
__device__ __forceinline__ unsigned pk_bf16(f32x2 v) { return __builtin_bit_cast(unsigned, __builtin_convertvector(v, bf16x2)); }
__device__ __forceinline__ unsigned pk_f16(float a, float b) { f32x2 v = {a, b}; return __builtin_bit_cast(unsigned, __builtin_convertvector(v, f16x2)); }
__device__ __forceinline__ unsigned pk_f16(f32x2 v) { return __builtin_bit_cast(unsigned, __builtin_convertvector(v, f16x2)); }
__device__ __forceinline__ f32x2 unpk_bf16(unsigned v) { return (f32x2){bflo(v), bfhi(v)}; }
__device__ __forceinline__ float bfr(float a) { return bflo(pk_bf16(a, a)); }
__device__ __forceinline__ void glds16(const void* sbase, unsigned voff, unsigned ldsaddr) {
  asm volatile("s_mov_b32 m0, %2\n\ts_nop 3\n\tglobal_load_lds_dwordx4 %0, %1" ::"v"(voff), "s"(sbase), "s"(ldsaddr) : "memory");
}
__device__ __forceinline__ unsigned ldsa(ldsp p) { return (unsigned)(size_t)p; }
__device__ __forceinline__ ldsp ldsq(unsigned a) { return (ldsp)(size_t)a; }
__device__ __forceinline__ s16x4 trread(unsigned a) { typedef short v4i16_t __attribute__((ext_vector_type(4))); return __builtin_bit_cast(s16x4, __builtin_amdgcn_ds_read_tr16_b64_v4i16((LAS v4i16_t*)(size_t)a)); }
__device__ __forceinline__ s16x8 cat8(s16x4 a, s16x4 b) { return __builtin_shufflevector(a, b, 0, 1, 2, 3, 4, 5, 6, 7); }
#define WAIT_V(n) asm volatile("s_waitcnt vmcnt(" #n ")" ::: "memory")
#define WAIT_L(n) asm volatile("s_waitcnt lgkmcnt(" #n ")" ::: "memory")
#define BAR_L() do { asm volatile("" ::: "memory"); __builtin_amdgcn_s_waitcnt(0xC07F); __builtin_amdgcn_s_barrier(); asm volatile("" ::: "memory"); } while (0)
#define SCHED() __builtin_amdgcn_sched_barrier(0)
#define OPAQUE(x) asm volatile("" : "+v"(x))
__device__ __forceinline__ f32x4 mfma_bf16(s16x8 a, s16x8 b, f32x4 c) { return __builtin_amdgcn_mfma_f32_16x16x32_bf16(__builtin_bit_cast(bf16x8, a), __builtin_bit_cast(bf16x8, b), c, 0, 0, 0); }
__device__ __forceinline__ f32x4 mfma_f16(s16x8 a, s16x8 b, f32x4 c) { return __builtin_amdgcn_mfma_f32_16x16x32_f16(__builtin_bit_cast(f16x8, a), __builtin_bit_cast(f16x8, b), c, 0, 0, 0); }
__device__ __forceinline__ u64 ld_agent(const void* p) { return __hip_atomic_load((const GAS u64*)p, __ATOMIC_RELAXED, __HIP_MEMORY_SCOPE_AGENT); }
__device__ __forceinline__ void st_agent(void* p, u64 v) { __hip_atomic_store((GAS u64*)p, v, __ATOMIC_RELAXED, __HIP_MEMORY_SCOPE_AGENT); }
__device__ __forceinline__ u64 ld_agent_async(const void* sbase) { u64 r; asm volatile("s_nop 4\n\tglobal_load_dwordx2 %0, %1, %2 sc1" : "=v"(r) : "v"(0u), "s"(sbase) : "memory"); return r; }
__device__ __forceinline__ u64 ld_agent_async_v(const void* sbase, unsigned voff) { u64 r; asm volatile("s_nop 4\n\tglobal_load_dwordx2 %0, %1, %2 sc1" : "=v"(r) : "v"(voff), "s"(sbase) : "memory"); return r; }
#define WAIT_V_TIE(n, x) asm volatile("s_waitcnt vmcnt(" #n ")" : "+v"(x)::"memory")
__device__ __forceinline__ void touch(const void* sbase, unsigned voff, unsigned ldsscratch) {
  asm volatile("s_mov_b32 m0, %2\n\ts_nop 3\n\tglobal_load_lds_dword %0, %1" ::"v"(voff), "s"(sbase), "s"(ldsscratch) : "memory");
}
__device__ __forceinline__ void glds16_dev(const void* sbase, unsigned voff, unsigned ldsaddr) {
  asm volatile("s_mov_b32 m0, %2\n\ts_nop 3\n\tglobal_load_lds_dwordx4 %0, %1 sc1" ::"v"(voff), "s"(sbase), "s"(ldsaddr) : "memory");
}
#define BAR_VL(n) do { asm volatile("s_waitcnt vmcnt(" #n ")" ::: "memory"); BAR_L(); } while (0)
constexpr unsigned SPIN_CAP = 1u << 22;
#define TMO(ws, spins, code)

template <int CTRL> __device__ __forceinline__ float dppf(float v) { return __builtin_bit_cast(float, __builtin_amdgcn_update_dpp(0, __builtin_bit_cast(int, v), CTRL, 0xf, 0xf, true)); }
__device__ __forceinline__ float red16(float s) { s += dppf<0x128>(s); s += dppf<0x124>(s); s += dppf<0x122>(s); s += dppf<0x121>(s); return s; }

#define STDECL(n)
#define STR(n, i)
#define STC(n, i)
#define STFLUSH(n, ws, kid, wv)
#define STAMPARG
#define STAMPPASS(n)
#define AST(i) STR(stx, i)
#define ASTC(i) STC(stx, i)

#define AMPDECL
#define AMP(i)
#define AMPEND()
constexpr int KG_REC = 136;
constexpr int KG_TAB = 128 * KG_REC;
constexpr int KC_BYTES = 1296;
constexpr int CV_KSTR = 4160;
constexpr int CV_U = 4 * CV_KSTR;
constexpr int CV_UB = 4608;
constexpr int CV_RED = 32768;

__device__ __forceinline__ f32x2 cmul(f32x2 a, f32x2 b) {
  f32x2 t, r;
  asm("v_pk_mul_f32 %0, %1, %2 op_sel:[0,0] op_sel_hi:[0,1]" : "=v"(t) : "v"(a), "v"(b));
  asm("v_pk_fma_f32 %0, %1, %2, %3 op_sel:[1,1,0] op_sel_hi:[1,0,1] neg_lo:[0,1,0]" : "=v"(r) : "v"(a), "v"(b), "v"(t));
  return r;
}
__device__ __forceinline__ void cmul4(f32x2 (&r)[4], const f32x2 (&a)[4], const f32x2 (&b)[4]) {
  asm("v_pk_mul_f32 %0, %4, %8 op_sel:[0,0] op_sel_hi:[0,1]\n\tv_pk_mul_f32 %1, %5, %9 op_sel:[0,0] op_sel_hi:[0,1]\n\t"
      "v_pk_mul_f32 %2, %6, %10 op_sel:[0,0] op_sel_hi:[0,1]\n\tv_pk_mul_f32 %3, %7, %11 op_sel:[0,0] op_sel_hi:[0,1]\n\t"
      "v_pk_fma_f32 %0, %4, %8, %0 op_sel:[1,1,0] op_sel_hi:[1,0,1] neg_lo:[0,1,0]\n\tv_pk_fma_f32 %1, %5, %9, %1 op_sel:[1,1,0] op_sel_hi:[1,0,1] neg_lo:[0,1,0]\n\t"
      "v_pk_fma_f32 %2, %6, %10, %2 op_sel:[1,1,0] op_sel_hi:[1,0,1] neg_lo:[0,1,0]\n\tv_pk_fma_f32 %3, %7, %11, %3 op_sel:[1,1,0] op_sel_hi:[1,0,1] neg_lo:[0,1,0]"
      : "=&v"(r[0]), "=&v"(r[1]), "=&v"(r[2]), "=&v"(r[3])
      : "v"(a[0]), "v"(a[1]), "v"(a[2]), "v"(a[3]), "v"(b[0]), "v"(b[1]), "v"(b[2]), "v"(b[3]));
}
__device__ __forceinline__ void cmul4h(u32x4& r, const unsigned (&a)[4], const unsigned (&b)[4]) {
  unsigned r0, r1, r2, r3;
  asm("v_pk_mul_f16 %0, %4, %8 op_sel:[0,0] op_sel_hi:[0,1]\n\tv_pk_mul_f16 %1, %5, %9 op_sel:[0,0] op_sel_hi:[0,1]\n\t"
      "v_pk_mul_f16 %2, %6, %10 op_sel:[0,0] op_sel_hi:[0,1]\n\tv_pk_mul_f16 %3, %7, %11 op_sel:[0,0] op_sel_hi:[0,1]\n\t"
      "v_pk_fma_f16 %0, %4, %8, %0 op_sel:[1,1,0] op_sel_hi:[1,0,1] neg_lo:[0,1,0]\n\tv_pk_fma_f16 %1, %5, %9, %1 op_sel:[1,1,0] op_sel_hi:[1,0,1] neg_lo:[0,1,0]\n\t"
      "v_pk_fma_f16 %2, %6, %10, %2 op_sel:[1,1,0] op_sel_hi:[1,0,1] neg_lo:[0,1,0]\n\tv_pk_fma_f16 %3, %7, %11, %3 op_sel:[1,1,0] op_sel_hi:[1,0,1] neg_lo:[0,1,0]\n\ts_nop 1"
      : "=&v"(r0), "=&v"(r1), "=&v"(r2), "=&v"(r3)
      : "v"(a[0]), "v"(a[1]), "v"(a[2]), "v"(a[3]), "v"(b[0]), "v"(b[1]), "v"(b[2]), "v"(b[3]));
  r = (u32x4){r0, r1, r2, r3};
}
__device__ __forceinline__ float exp_acc(float x) {
  const float t = x * 1.44269504f, r = fmaf(x, 1.44269504f, -t) + x * 1.925963e-08f;
  const float e = __builtin_amdgcn_exp2f(t);
  return fmaf(e * 0.69314718f, r, e);
}
__device__ __forceinline__ float rdl(float s, int l) { return __builtin_bit_cast(float, __builtin_amdgcn_readlane(__builtin_bit_cast(int, s), l)); }
__device__ __forceinline__ float wave_sum(float s) { s = red16(s); return (rdl(s, 0) + rdl(s, 16)) + (rdl(s, 32) + rdl(s, 48)); }
template <int CTRL> __device__ __forceinline__ unsigned dppu(unsigned v) { return (unsigned)__builtin_amdgcn_update_dpp(0, (int)v, CTRL, 0xf, 0xf, true); }
struct ConvOrder {
  int tt[36], cc[36], ld[24];
  constexpr ConvOrder() : tt{}, cc{}, ld{} {
    int n = 0, nl = 0; bool sb[8] = {};
    for (int t = 0; t < 8; ++t)
      for (int c = t; c < 8; ++c) {
        if (c == t) { ld[nl++] = 8 + t; ld[nl++] = 16 + t; }
        tt[n] = t; cc[n] = c; ++n;
        if (!sb[c - t]) { sb[c - t] = true; ld[nl++] = c - t; }
      }
  }
};
struct AuxIn { const u16* logA; const u16* Aim; const u16* Cre; const u16* Cim; const u16* logdt; const u16* dskip; };
struct AuxPar { unsigned la, ai, cr, ci, c2r, c2i, ldt, dsk; };
struct AuxCh { float ksc, dsk; };
typedef const __attribute__((address_space(4))) unsigned* cptr32;
__device__ __forceinline__ void aux_load(AuxPar& p, const AuxIn& in, int ch, int v, int lane) {
  const unsigned o2 = (ch * NM + 32 * v + (lane & 31)) * 2, o4 = ch * NM * 2 + lane * 4;
  asm volatile("s_nop 4\n\tglobal_load_ushort %0, %6, %8\n\tglobal_load_ushort %1, %6, %9\n\tglobal_load_ushort %2, %6, %10\n\tglobal_load_ushort %3, %6, %11\n\t"
               "global_load_dword %4, %7, %10\n\tglobal_load_dword %5, %7, %11"
               : "=&v"(p.la), "=&v"(p.ai), "=&v"(p.cr), "=&v"(p.ci), "=&v"(p.c2r), "=&v"(p.c2i)
               : "v"(o2), "v"(o4), "s"(in.logA), "s"(in.Aim), "s"(in.Cre), "s"(in.Cim) : "memory");
  p.ldt = (((cptr32)(size_t)in.logdt)[ch >> 1] >> (16 * (ch & 1))) & 0xffffu;
  p.dsk = (((cptr32)(size_t)in.dskip)[ch >> 1] >> (16 * (ch & 1))) & 0xffffu;
}
#define AUX_WAIT(n, p) asm volatile("s_waitcnt vmcnt(" #n ")" : "+v"((p).la), "+v"((p).ai), "+v"((p).cr), "+v"((p).ci), "+v"((p).c2r), "+v"((p).c2i)::"memory")

template <class PRE>
__device__ __forceinline__ void kgen4(const AuxPar& par, AuxCh& o, unsigned tab, unsigned kc, int v, int lane STAMPARG, PRE pre_barrier) {
  const int i = lane & 15, g = lane >> 4, hf = lane >> 5, t = v * 64 + lane;
  const float dt = bfr(exp_acc(bf2f((u16)par.ldt)));
  unsigned cm = max(max(par.c2r & 0x7fffu, (par.c2r >> 16) & 0x7fffu), max(par.c2i & 0x7fffu, (par.c2i >> 16) & 0x7fffu));
  cm = max(cm, dppu<0x128>(cm)); cm = max(cm, dppu<0x124>(cm)); cm = max(cm, dppu<0x122>(cm)); cm = max(cm, dppu<0x121>(cm));
  cm = max(max((unsigned)__builtin_amdgcn_readlane(cm, 0), (unsigned)__builtin_amdgcn_readlane(cm, 16)), max((unsigned)__builtin_amdgcn_readlane(cm, 32), (unsigned)__builtin_amdgcn_readlane(cm, 48)));
  unsigned ce = cm >> 7; ce = ce < 20u ? 20u : (ce > 230u ? 230u : ce);
  const float s = __uint_as_float((253u - ce) << 23);
  o.ksc = dt * __uint_as_float((ce + 1u - 16u) << 23); o.dsk = bf2f((u16)par.dsk) * (1.0f / 1024.0f);
  {
    const float zr = -bfr(exp_acc(bf2f((u16)par.la))) * dt, zi = bf2f((u16)par.ai) * dt;
    const float a2 = zr * 1.44269504088896341f;
    const float rh = zi * 0.15915494f, rl = fmaf(zi, 0.15915494f, -rh) + zi * 6.4206383e-09f;
    const float p = hf ? 64.0f : 1.0f, ps = hf ? -64.0f : 1.0f;
    const float e = __builtin_amdgcn_exp2f(a2 * p), tt = rh * ps, ph = (tt - __builtin_rintf(tt)) + rl * ps;
    const f32x2 b1 = {e * __builtin_amdgcn_cosf(ph), e * __builtin_amdgcn_sinf(ph)};
    const float sc = 2.0f * s; const f32x2 C = {hf ? bf2f((u16)par.cr) * sc : 1.0f, hf ? -bf2f((u16)par.ci) * sc : 0.0f};
    const f32x2 b2 = cmul(b1, b1), b3 = cmul(b2, b1), b4 = cmul(b2, b2);
    const f32x2 b8 = cmul(b4, b4), A1 = cmul(C, b4);
    f32x2 q[4];
    { const f32x2 a[4] = {b8, b8, C, C}, b[4] = {b4, b8, b8, b4}; cmul4(q, a, b); }
    const f32x2 b12 = q[0], b16 = q[1], A2 = q[2];
    { const f32x2 a[4] = {b16, C, C, A1}, b[4] = {b16, b12, b16, b16}; cmul4(q, a, b); }
    const f32x2 b32 = q[0], A3 = q[1], B0 = q[2], B1 = q[3];
    const f32x2 B2 = cmul(A2, b16), B3 = cmul(A3, b16);
    const unsigned rec = tab + (32 * v + (lane & 31)) * KG_REC;
    if (hf == 0) {
      LAS f32x2* r2 = (LAS f32x2*)(size_t)rec;
      r2[0] = C; r2[1] = A1; r2[2] = A2; r2[3] = A3; r2[4] = b1; r2[5] = b2; r2[6] = b3; r2[7] = b4;
      *(LAS u32x2*)(size_t)(rec + 112) = (u32x2){pk_f16(b16), pk_f16(b32)};
    } else {
      LAS u32x2* r2 = (LAS u32x2*)(size_t)(rec + 64);
      r2[0] = (u32x2){0x00003c00u, pk_f16(b1)}; r2[1] = (u32x2){pk_f16(b2), pk_f16(b3)};
      r2[2] = (u32x2){pk_f16(C), pk_f16(B0)}; r2[3] = (u32x2){pk_f16(A1), pk_f16(B1)}; r2[4] = (u32x2){pk_f16(A2), pk_f16(B2)}; r2[5] = (u32x2){pk_f16(A3), pk_f16(B3)};
    }
  }
  AST(1);
  {
    f32x4 acc[2][4];
#pragma unroll
    for (int e = 0; e < 2; ++e)
#pragma unroll
      for (int qv = 0; qv < 4; ++qv) acc[e][qv] = (f32x4){0.f, 0.f, 0.f, 0.f};
    const unsigned rb = tab + (32 * v + g) * KG_REC;
    unsigned ra = rb + 8 * (i >> 2), rtb = rb + 32 + 8 * (i & 3), rub = rb + 64 + 4 * (i & 3), rcc = rb + 80 + 8 * (i >> 2), rw = rb + 112;
    OPAQUE(ra); OPAQUE(rtb); OPAQUE(rub); OPAQUE(rcc); OPAQUE(rw);
    f32x2 ta[2][4], tb[2][4]; unsigned ub[2][4]; u32x2 cc[2][4], ww[2][4];
#pragma unroll
    for (int ks = 0; ks < 2; ++ks)
#pragma unroll
      for (int jm = 0; jm < 4; ++jm) {
        const int o2 = (16 * ks + 4 * jm) * KG_REC;
        ta[ks][jm] = *(const LAS f32x2*)(size_t)(ra + o2); tb[ks][jm] = *(const LAS f32x2*)(size_t)(rtb + o2); ub[ks][jm] = *(const LAS unsigned*)(size_t)(rub + o2);
        cc[ks][jm] = *(const LAS u32x2*)(size_t)(rcc + o2); ww[ks][jm] = *(const LAS u32x2*)(size_t)(rw + o2);
      }
    SCHED();
#pragma unroll
    for (int ks = 0; ks < 2; ++ks) {
      f32x2 y0[4]; unsigned y0h[4], c0[4], c1[4], w16[4], w32[4];
      cmul4(y0, ta[ks], tb[ks]);
#pragma unroll
      for (int jm = 0; jm < 4; ++jm) { y0h[jm] = pk_f16(y0[jm]); c0[jm] = cc[ks][jm][0]; c1[jm] = cc[ks][jm][1]; w16[jm] = ww[ks][jm][0]; w32[jm] = ww[ks][jm][1]; }
      u32x4 X[2], Y[4];
      cmul4h(X[0], c0, ub[ks]); cmul4h(X[1], c1, ub[ks]);
      Y[0] = (u32x4){y0h[0], y0h[1], y0h[2], y0h[3]};
      cmul4h(Y[1], y0h, w16); cmul4h(Y[2], y0h, w32);
      { const unsigned y1[4] = {Y[1][0], Y[1][1], Y[1][2], Y[1][3]}; cmul4h(Y[3], y1, w32); }
#pragma unroll
      for (int qv = 0; qv < 4; ++qv)
#pragma unroll
        for (int e = 0; e < 2; ++e) acc[e][qv] = mfma_f16(__builtin_bit_cast(s16x8, X[e]), __builtin_bit_cast(s16x8, Y[qv]), acc[e][qv]);
      SCHED();
    }
    const unsigned pt = tab + v * 32 * KG_REC;
    const int sh = g == 0 ? 0 : g == 1 ? 1 : 2;
    const unsigned pidx0 = pt + 4 * ((g == 0 ? 0 : g == 1 ? 256 : 384) + (((g == 3 ? 256 : 0) + i + 1) >> sh)), pu0 = 4 * (16 >> sh);
    const unsigned pidx1 = pt + 4 * (512 + 32 * g + ((i + 1) >> 3));
    if (((i + 1) & ((1 << sh) - 1)) == 0) {
#pragma unroll
      for (int qv = 0; qv < 4; ++qv)
#pragma unroll
        for (int r = 0; r < 4; ++r) *(LAS float*)(size_t)(pidx0 + (4 * r + qv) * pu0) = acc[0][qv][r];
    }
    if ((i & 7) == 7) {
#pragma unroll
      for (int qv = 0; qv < 4; ++qv)
#pragma unroll
        for (int r = 0; r < 4; ++r) *(LAS float*)(size_t)(pidx1 + 4 * (8 * r + 2 * qv)) = acc[1][qv][r];
    }
  }
  AST(2);
  pre_barrier();
  BAR_L();
  float pv[3][4];
#pragma unroll
  for (int k = 0; k < 3; ++k) { const int id = t + 256 * k < 641 ? t + 256 * k : 641;
#pragma unroll
    for (int q = 0; q < 4; ++q) pv[k][q] = *(const LAS float*)(size_t)(tab + q * 32 * KG_REC + 4 * id); }
  SCHED();
#pragma unroll
  for (int k = 0; k < 3; ++k) {
    const int id = t + 256 * k;
    float val = (pv[k][0] + pv[k][1]) + (pv[k][2] + pv[k][3]);
    if (k == 0 && v == 0) { const float k0 = wave_sum(bflo(par.c2r) + bfhi(par.c2r)); if (id == 0) val = 2.0f * s * k0; }
    if (id >= 641) val = 0.f;
    if (id <= 641) *(LAS _Float16*)(size_t)(kc + 2 * id) = (_Float16)(val * 64.0f);
  }
  AST(3);
}

__device__ __forceinline__ void conv_issue_u(const char* ws, int ch, unsigned ub, int v, int lane) {
  glds16_dev(ws + WS_UT + (size_t)ch * (L_ * 2) + v * 1024, lane * 16, ub + 512 + v * 1024);
}
__device__ __forceinline__ void conv_expand(unsigned kc, unsigned cv, unsigned ub, int v, int lane) {
  const int t = v * 64 + lane;
  unsigned ga[12];
  const unsigned gb = kc - 2 * 17024;
#pragma unroll
  for (int k = 0; k < 12; k += 2) {
    const int l = 2047 - 8 * t - k;
    const unsigned pk = pk_bf16((float)l, (float)(l - 1));
    ga[k] = gb + 2 * (pk & 0xffffu); ga[k + 1] = gb + 2 * (pk >> 16);
  }
  if (v == 3) {
#pragma unroll
    for (int k = 0; k < 11; ++k) { const int l = 2047 - 8 * t - k; if (l < 128) ga[k] = kc + 2 * (l < 0 ? 641 : l); }
  }
  unsigned e[11];
#pragma unroll
  for (int k = 0; k < 11; ++k) e[k] = *(const LAS u16*)(size_t)ga[k];
  if (t < 32) *(LAS u32x4*)(size_t)(ub + t * 16) = (u32x4){0u, 0u, 0u, 0u};
  else if (t < 48) *(LAS u32x4*)(size_t)(cv + ((t - 32) >> 2) * CV_KSTR + 4096 + (t & 3) * 16) = (u32x4){0u, 0u, 0u, 0u};
  SCHED();
#pragma unroll
  for (int sg = 0; sg < 4; ++sg) {
    u32x4 o;
#pragma unroll
    for (int q = 0; q < 4; ++q) o[q] = e[2 * q + 3 - sg] | (e[2 * q + 4 - sg] << 16);
    *(LAS u32x4*)(size_t)(cv + sg * CV_KSTR + t * 16) = o;
  }
}
__device__ __forceinline__ void conv_mm(unsigned cv, unsigned ub, unsigned red, int v, int lane) {
  f32x4 acc[8];
  const int i = lane & 15, g = lane >> 4;
  unsigned pal = cv + (i & 3) * CV_KSTR + (2028 - 4 * (i >> 2) + 8 * g) * 2 - 3648 - 2 * v * 64, pah = pal + 8;
  unsigned pb = ub + 512 + (16 * i - 16 + 8 * g) * 2 - 64 - 2 * v * 64;
  OPAQUE(pal); OPAQUE(pah); OPAQUE(pb);
  s16x4 AL[2][8], AH[2][8]; s16x8 B[2][8];
  constexpr ConvOrder O{};
  auto ld1 = [&](int set, int code) {
    const int k = code & 7;
    if (code < 8) B[set][k] = *(const LAS s16x8*)(size_t)(pb + 64 + k * 512 - set * 64);
    else if (code < 16) AL[set][k] = *(const LAS s16x4*)(size_t)(pal + 3648 - k * 512 - set * 64);
    else AH[set][k] = *(const LAS s16x4*)(size_t)(pah + 3648 - k * 512 - set * 64);
  };
#pragma unroll
  for (int c = 0; c < 8; ++c) acc[c] = (f32x4){0.f, 0.f, 0.f, 0.f};
#pragma unroll
  for (int r = 0; r < 24; ++r) { ld1(0, O.ld[r]); SCHED(); }
#pragma unroll
  for (int set = 0; set < 2; ++set)
#pragma unroll
    for (int n = 0; n < 36; ++n) {
      acc[O.cc[n]] = mfma_f16(cat8(AL[set][O.tt[n]], AH[set][O.tt[n]]), B[set][O.cc[n] - O.tt[n]], acc[O.cc[n]]);
      if (set == 0 && n < 24) ld1(1, O.ld[n]);
      if (set == 0) SCHED();
    }
#pragma unroll
  for (int c = 0; c < 8; ++c) *(LAS f32x4*)(size_t)(red + ((((c >> 1) * 4 + v) * 2 + (c & 1)) << 10) + lane * 16) = acc[c];
}
__device__ __forceinline__ void conv_finish(const AuxCh& o, char* ws, int ch, bool store, unsigned ub, unsigned red, int v, int lane) {
  const int i = lane & 15, g = lane >> 4;
  f32x4 ot[4][2]; u32x2 uq[2];
  const int l0_ = 512 * v + 16 * i + 4 * g;
#pragma unroll
  for (int cc = 0; cc < 2; ++cc) {
    uq[cc] = *(const LAS u32x2*)(size_t)(ub + 512 + 2 * (l0_ + 256 * cc));
#pragma unroll
    for (int si = 0; si < 4; ++si) ot[si][cc] = *(const LAS f32x4*)(size_t)(red + (((v * 4 + si) * 2 + cc) << 10) + lane * 16);
  }
  SCHED();
  const auto rs = __builtin_amdgcn_make_buffer_rsrc((u16*)(ws + WS_T1) + (size_t)ch * L_, 0, store ? L_ * 2 : 0, 0x00020000);
#pragma unroll
  for (int cc = 0; cc < 2; ++cc) {
    const f32x4 y = (ot[0][cc] + ot[1][cc]) + (ot[2][cc] + ot[3][cc]);
    const unsigned ua = uq[cc][0], ub2 = uq[cc][1];
    const f16x2 u0 = __builtin_bit_cast(f16x2, ua), u1 = __builtin_bit_cast(f16x2, ub2);
    const f32x2 s0 = (f32x2){(float)u0.x, (float)u0.y} * o.dsk, s1 = (f32x2){(float)u1.x, (float)u1.y} * o.dsk;
    const f32x2 ks = {o.ksc, o.ksc};
    const u32x2 ov = {pk_bf16(__builtin_elementwise_fma((f32x2){y[0], y[1]}, ks, s0)), pk_bf16(__builtin_elementwise_fma((f32x2){y[2], y[3]}, ks, s1))};
    __builtin_amdgcn_raw_buffer_store_b64(ov, rs, 2 * (l0_ + 256 * cc), 0, 16);
  }
}
__device__ __forceinline__ void conv_publish(char* ws, u64 tag, int ch, bool store, int v, int lane) {
  if (store && lane == 0) st_agent(ws + WS_CHRDY + (size_t)(ch * 4 + v) * 8, tag);
}
__device__ __forceinline__ float dot2_ones(unsigned v, float acc) { return __builtin_amdgcn_fdot2_f32_bf16(__builtin_bit_cast(bf16x2, v), __builtin_bit_cast(bf16x2, 0x3f803f80u), acc, false); }
constexpr int LN_STR = 1312;
__device__ __forceinline__ u64 ln_block(const u16* __restrict__ x, char* ws, int blk, int b, ldsp lds STAMPARG) {
  const int tid = threadIdx.x, w = tid >> 6, lane = tid & 63, rq = lane >> 4, q = lane & 15;
  const int tok0 = blk * 64;
  u32x4 v[2][5];
#pragma unroll
  for (int p = 0; p < 2; ++p)
#pragma unroll
    for (int i = 0; i < 5; ++i) v[p][i] = *(const u32x4*)(x + (size_t)(tok0 + w * 8 + p * 4 + rq) * D_ + (q + 16 * i) * 8);
  const u64 tag = ld_agent(ws + WS_NONCE) + 1;
  if (tid == 0) st_agent(ws + WS_ARR + b * 8, tag);
  AST(0);
  float mb[2], rinv[2];
#pragma unroll
  for (int p = 0; p < 2; ++p) {
    float s0 = 0.f, s1 = 0.f;
#pragma unroll
    for (int i = 0; i < 5; ++i)
#pragma unroll
      for (int j = 0; j < 4; j += 2) { s0 = dot2_ones(v[p][i][j], s0); s1 = dot2_ones(v[p][i][j + 1], s1); }
    const float mean = red16(s0 + s1) * (1.0f / 640.0f);
    f32x2 q2 = {0.f, 0.f};
#pragma unroll
    for (int i = 0; i < 5; ++i)
#pragma unroll
      for (int j = 0; j < 4; ++j) { const f32x2 a = unpk_bf16(v[p][i][j]) - mean; q2 = __builtin_elementwise_fma(a, a, q2); }
    const float var = red16(q2.x + q2.y) * (1.0f / 640.0f);
    mb[p] = bfr(mean); rinv[p] = __builtin_amdgcn_rcpf(bfr(__builtin_amdgcn_sqrtf(bfr(bfr(var) + 1e-5f))));
    if (q == 0) st_agent(ws + WS_STATS + (size_t)(tok0 + w * 8 + p * 4 + rq) * 8, ((u64)__float_as_uint(rinv[p]) << 32) | __float_as_uint(mb[p]));
  }
  AST(1);
#pragma unroll
  for (int p = 0; p < 2; ++p) {
    const int lrow = w * 8 + p * 4 + rq;
    const float r1024 = rinv[p] * 1024.0f;
#pragma unroll
    for (int i = 0; i < 5; ++i) {
      u32x4 o;
#pragma unroll
      for (int j = 0; j < 4; ++j) {
        o[j] = pk_f16((unpk_bf16(v[p][i][j]) - mb[p]) * r1024);
      }
      *(LAS u32x4*)(lds + lrow * LN_STR + (q + 16 * i) * 16) = o;
    }
  }
  AST(3);
  BAR_L();
  AST(4);
  const auto rs = __builtin_amdgcn_make_buffer_rsrc((void*)(ws + WS_UT), 0, D_ * L_ * 2, 0x00020000);
  const int g = lane >> 4, qq = (lane & 15) >> 2, pp = lane & 3, ci = lane & 15;
  const unsigned t2 = ldsa(lds) + 64 * LN_STR + w * 2048;
#pragma unroll
  for (int k = 0; k < 5; ++k) {
    const int cb = w + 8 * k;
#pragma unroll
    for (int th = 0; th < 2; ++th) {
      const unsigned a = ldsa(lds) + (th * 32 + 8 * g + qq) * LN_STR + cb * 32 + pp * 8;
      const s16x4 lo = trread(a), hi = trread(a + 4 * LN_STR);
      *(LAS s16x8*)(size_t)(t2 + ci * 128 + (((4 * th + g) ^ (ci & 7)) << 4)) = cat8(lo, hi);
    }
#pragma unroll
    for (int hh = 0; hh < 2; ++hh) {
      const int cr = 8 * hh + (lane >> 3), o = lane & 7;
      const u32x4 v4 = *(const LAS u32x4*)(size_t)(t2 + cr * 128 + ((o ^ (cr & 7)) << 4));
      __builtin_amdgcn_raw_buffer_store_b128(v4, rs, ((cb * 16 + cr) * L_ + tok0 + 8 * o) * 2, 0, 16);
    }
  }
  AST(5);
  BAR_VL(0);
  AST(6);
  if (tid == 0) st_agent(ws + WS_LNRDY + (blk >> 2) * 128 + (blk & 3) * 8, tag);
  if (tid == 64) st_agent(ws + WS_UTRDY + blk * 8, tag);
  return tag;
}

constexpr int G1_SLOT = 49152, G1_W = 32768, G1_CS = 3 * G1_SLOT, KA_LDS = G1_CS + 1024;
struct G1Ctx { const char* xs; const char* wg; const char* wu; unsigned xo[4]; unsigned wo; unsigned ldx; unsigned ldw; };
__device__ __forceinline__ void g1_setup(G1Ctx& c, const u16* x, const u16* w1, const u16* w3, int mt, int nu, ldsp lds, int w, int lane) {
  c.xs = (const char*)(x + (size_t)mt * 256 * D_); c.wg = (const char*)(w1 + nu * 64); c.wu = (const char*)(w3 + nu * 64);
#pragma unroll
  for (int j = 0; j < 4; ++j) { const int row = 32 * w + 8 * j + (lane >> 3); c.xo[j] = row * (D_ * 2) + (((lane & 7) ^ ((row >> 1) & 7)) << 4); }
  { const int row = 8 * w + (lane >> 3), ph = ((row >> 1) & 1) + 2 * ((row >> 3) & 1); c.wo = row * (F_ * 2) + (((lane & 7) ^ (2 * ph)) << 4); }
  c.ldx = ldsa(lds) + w * 4096; c.ldw = ldsa(lds) + G1_W + w * 1024;
}
__device__ __forceinline__ void g1_issue1(const G1Ctx& c, int kt, int slot, int j) {
  if (j < 4) glds16(c.xs + kt * 128, c.xo[j], c.ldx + slot * G1_SLOT + j * 1024);
  else if (j == 4) glds16(c.wg + (size_t)kt * 64 * F_ * 2, c.wo, c.ldw + slot * G1_SLOT);
  else glds16(c.wu + (size_t)kt * 64 * F_ * 2, c.wo, c.ldw + slot * G1_SLOT + 8192);
}
__device__ __forceinline__ void g1_issue(const G1Ctx& c, int kt, int slot) {
#pragma unroll
  for (int j = 0; j < 6; ++j) g1_issue1(c, kt, slot, j);
}
template <class LATE>
__device__ __forceinline__ void g1_main(f32x4 (&acc)[4][4], f32x4& cs, u64 (&st)[4], bool& ut_ok, LATE late, const G1Ctx& c, ldsp lds, int w, int lane, const char* ws, int mt, u64 tag) {
  const int wm = w >> 1, wn = w & 1, fr = lane & 15, g = lane >> 4;
  const int psi = (fr >> 1) & 7;
  const int q = fr >> 2, p = lane & 3, phi = ((q >> 1) & 1) + 2 * (g & 1);
  unsigned xa[3][2], wa[3][4];
#pragma unroll
  for (int sl = 0; sl < 3; ++sl) {
#pragma unroll
    for (int ks = 0; ks < 2; ++ks) { xa[sl][ks] = ldsa(lds) + sl * G1_SLOT + (64 * wm + fr) * 128 + (((4 * ks + g) ^ psi) << 4); OPAQUE(xa[sl][ks]); }
#pragma unroll
    for (int s = 0; s < 4; ++s) { const int b = s ^ wm; const int nb = 2 * wn + (b & 1);
      wa[sl][s] = ldsa(lds) + sl * G1_SLOT + G1_W + (b >> 1) * 8192 + (8 * g + q) * 128 + ((2 * (nb ^ phi) + (p >> 1)) << 4) + 8 * (p & 1); OPAQUE(wa[sl][s]); }
  }
  s16x8 XF[2][4]; s16x4 WL[2][4], WH[2][4];
  const s16x8 ones = {0x3f80, 0x3f80, 0x3f80, 0x3f80, 0x3f80, 0x3f80, 0x3f80, 0x3f80};
  auto rd1 = [&](int set, int slot, int ks, int r) {
    const int code[12] = {0, 1, 8, 2, 3, 4, 5, 6, 7, 9, 10, 11}; r = code[r];
    if (r < 8) { const int s = r >> 1; if (r & 1) WH[set][s] = trread(wa[slot][s] + ks * 4096 + 512); else WL[set][s] = trread(wa[slot][s] + ks * 4096); }
    else XF[set][r - 8] = *(const LAS s16x8*)(size_t)(xa[slot][ks] + (r - 8) * 2048);
  };
  auto mm1 = [&](int set, int i) { const int m = i >> 2, s = i & 3; acc[s][m] = mfma_bf16(cat8(WL[set][s], WH[set][s]), XF[set][m], acc[s][m]); };
  auto half = [&](int set, int rs, int rks, int dkt, int dslot, int d0, int d1) {
#pragma unroll
    for (int j = 0; j < 8; ++j) {
      mm1(set, 2 * j);
      if (rs >= 0 && j < 6) rd1(set ^ 1, rs, rks, 2 * j);
      mm1(set, 2 * j + 1);
      if (rs >= 0 && j < 6) rd1(set ^ 1, rs, rks, 2 * j + 1);
      if (dkt >= 0 && (j & 1) && d0 + (j >> 1) < d1) g1_issue1(c, dkt, dslot, d0 + (j >> 1));
      SCHED();
    }
    cs = mfma_bf16(cat8(WL[set][0], WH[set][0]), ones, cs); SCHED();
  };
#pragma unroll
  for (int s = 0; s < 4; ++s)
#pragma unroll
    for (int m = 0; m < 4; ++m) acc[s][m] = (f32x4){0.f, 0.f, 0.f, 0.f};
  cs = (f32x4){0.f, 0.f, 0.f, 0.f};
  const char* rdy = ws + WS_LNRDY + mt * 128 + (lane & 3) * 8;
  u64 flag = 0, flag2 = 0; bool early = false;
#pragma unroll
  for (int r = 0; r < 12; ++r) rd1(0, 0, 0, r);
  SCHED();
#pragma unroll
  for (int kt = 0; kt < 10; ++kt) {
    half(0, kt % 3, 1, (kt >= 1 && kt + 2 < 10) ? kt + 2 : -1, (kt + 2) % 3, 3, 6);
    if (kt < 9) {
      if (kt < 8) BAR_VL(6); else BAR_VL(0);
      SCHED();
      if (kt == 7) { flag = ld_agent(rdy); flag2 = ld_agent(ws + WS_UTRDY + (lane & 31) * 8); SCHED(); }
      if (kt == 8) {
        early = __all(flag == tag); ut_ok = __all(flag2 == tag);
#pragma unroll
        for (int m = 0; m < 4; ++m) st[m] = ld_agent(ws + WS_STATS + (size_t)(mt * 256 + 64 * wm + 16 * m + fr) * 8);
        late(ut_ok);
        SCHED();
      }
      half(1, (kt + 1) % 3, 0, kt + 3 < 10 ? kt + 3 : -1, kt % 3, 0, 3);
    } else half(1, -1, 0, -1, 0, 0, 0);
  }
  if (!early) {
    unsigned spins = 0;
    while (!__all(ld_agent(rdy) == tag) && ++spins < SPIN_CAP) __builtin_amdgcn_s_sleep(2);
    TMO(ws, spins, 1);
#pragma unroll
    for (int m = 0; m < 4; ++m) st[m] = ld_agent(ws + WS_STATS + (size_t)(mt * 256 + 64 * wm + 16 * m + fr) * 8);
  }
}
template <bool SW>
__device__ __forceinline__ void g1_epi_math(const f32x4 (&acc)[4][4], const LAS float* cst, const u64 (&st)[4], ldsp stg, int w, int lane) {
  const int wm = w >> 1, wn = w & 1, fr = lane & 15, g = lane >> 4;
  if (SW) asm volatile("; swapped epilogue begin"); else asm volatile("; plain epilogue begin");
#pragma unroll
  for (int sp = 0; sp < 2; ++sp) {
    const int j = sp ^ (wm & 1);
    const f32x4 cg = *(const LAS f32x4*)(cst + (wn * 4 + j) * 16 + 4 * g), cu = *(const LAS f32x4*)(cst + (wn * 4 + 2 + j) * 16 + 4 * g);
#pragma unroll
    for (int m = 0; m < 4; ++m) {
      const float mean = __uint_as_float((unsigned)st[m]), rinv = __uint_as_float((unsigned)(st[m] >> 32));
      const f32x4 ga = acc[SW ? sp + 2 : sp][m], ua = acc[SW ? sp : sp + 2][m];
      const float a1 = rinv * -1.44269504088896341f, sd = __builtin_amdgcn_rcpf(rinv), ia2 = sd * sd;
      u32x2 o;
#pragma unroll
      for (int hh = 0; hh < 2; ++hh) {
        const f32x2 nm = {-mean, -mean};
        const f32x2 tg = __builtin_elementwise_fma(nm, (f32x2){cg[2 * hh], cg[2 * hh + 1]}, (f32x2){ga[2 * hh], ga[2 * hh + 1]});
        const f32x2 tu = __builtin_elementwise_fma(nm, (f32x2){cu[2 * hh], cu[2 * hh + 1]}, (f32x2){ua[2 * hh], ua[2 * hh + 1]});
        const f32x2 ea = tg * a1;
        const f32x2 ee = {__builtin_amdgcn_exp2f(ea.x), __builtin_amdgcn_exp2f(ea.y)};
        const f32x2 dn = __builtin_elementwise_fma(ee, (f32x2){ia2, ia2}, (f32x2){ia2, ia2});
        const f32x2 rc = {__builtin_amdgcn_rcpf(dn.x), __builtin_amdgcn_rcpf(dn.y)};
        o[hh] = pk_bf16((tg * tu) * rc);
      }
      const int row = 64 * wm + 16 * m + fr, ch = 4 * wn + 2 * j + (g >> 1);
      *(LAS u32x2*)(stg + row * 128 + ((ch ^ (row & 7)) << 4) + 8 * (g & 1)) = o;
    }
  }
  if (SW) asm volatile("; swapped epilogue end" ::: "memory"); else asm volatile("; plain epilogue end" ::: "memory");
}

constexpr int G2_SLOT = 20480, G2_W = 12288, G2_R = 7;
constexpr int LD_CST = G1_CS;
constexpr int LD_KC = LD_CST + 1024;
constexpr int LD_TAB = 2 * G1_SLOT;
constexpr int LD_U = LD_KC + 2 * KC_BYTES;
constexpr int LD_RED = 32768;
constexpr int LD_CV = 98304;
constexpr int LD_XT = 143360;
constexpr int LD_BYTES = 163840;
constexpr int LD_SCR = LD_BYTES - 256;
constexpr int KCODE_LINES = 340;
static_assert(LD_TAB + 2 * KG_TAB <= G1_CS && LD_U % 16 == 0 && LD_U + 2 * CV_UB <= LD_BYTES && LD_RED + 2 * CV_RED <= LD_CV && LD_CV + 2 * CV_U <= G2_R * G2_SLOT && G2_R * G2_SLOT <= LD_XT && LD_XT + 12288 <= LD_BYTES, "lds map");

__host__ __device__ constexpr int g2_slot(int t) { return (t % 7) == 0 ? 0 : (t % 7) == 1 ? 5 : (t % 7) == 2 ? 6 : (t % 7) - 2; }
struct G2Ctx { const char* hs; const char* wsrc; unsigned ho[3], wo[2], lh, lw; };
struct G2Frag { s16x8 X[2][3]; s16x4 WL[2][2], WH[2][2]; };
__device__ __forceinline__ void g2_setup(G2Ctx& c, const char* ws, const u16* w2, int mt, int nu, unsigned l0, int lw, int lane) {
  c.hs = (const char*)((const u16*)(ws + WS_H) + (size_t)mt * 96 * F_); c.wsrc = (const char*)(w2 + nu * 64);
  c.lh = l0 + lw * 3072; c.lw = l0 + G2_W + lw * 2048;
#pragma unroll
  for (int j = 0; j < 3; ++j) {
    const int pc = 3 * lw + j, row = 8 * pc + (lane >> 3); int r0 = 8 * pc; r0 = mt * 96 + r0 < L_ - 8 ? r0 : L_ - 8 - mt * 96;
    c.ho[j] = (r0 + (lane >> 3)) * (F_ * 2) + (((lane & 7) ^ ((row >> 1) & 7)) << 4);
  }
#pragma unroll
  for (int j = 0; j < 2; ++j) { const int row = 8 * (2 * lw + j) + (lane >> 3), ph = ((row >> 1) & 1) + 2 * ((row >> 3) & 1); c.wo[j] = row * (D_ * 2) + (((lane & 7) ^ (2 * ph)) << 4); }
}
__device__ __forceinline__ void g2_issue_w(const G2Ctx& c, int kt, int slot) {
#pragma unroll
  for (int j = 0; j < 2; ++j) glds16(c.wsrc + (size_t)kt * (64 * D_ * 2), c.wo[j], c.lw + slot * G2_SLOT + j * 1024);
}
__device__ __forceinline__ void g2_issue_h(const G2Ctx& c, int kt, int slot) {
#pragma unroll
  for (int j = 0; j < 3; ++j) glds16_dev(c.hs + kt * 128, c.ho[j], c.lh + slot * G2_SLOT + j * 1024);
}
__device__ __forceinline__ unsigned g2_rdy_off(int mt, int lane) {
  const int a = (mt * 96) >> 8, b = (mt * 96 + 95 < L_ ? mt * 96 + 95 : L_ - 1) >> 8;
  return (unsigned)WS_HRDY + (lane < 32 ? a : b) * 256 + ((lane & 31) < 27 ? (lane & 31) : 0) * 8;
}
__device__ __forceinline__ void gemm2_tile(const u16* __restrict__ x, const u16* __restrict__ w2, char* ws, u16* __restrict__ out, u64 tag, int mt, int nu, G2Ctx& c, bool h01, int pub_ch, ldsp lds, int w, int lane STAMPARG) {
  const unsigned l0 = ldsa(lds);
  const int fr = lane & 15, g = lane >> 4;
  if (w >= 4) {
    const int lw = w - 4;
    if (!h01) {
      const char* rdy = ws + g2_rdy_off(mt, lane);
      unsigned spins = 0;
      while (!__all(ld_agent(rdy) == tag) && ++spins < SPIN_CAP) __builtin_amdgcn_s_sleep(1);
      TMO(ws, spins, 2);
    }
    AST(8);
    if (!h01) {
#pragma unroll
      for (int st = 0; st < 3; ++st) g2_issue_h(c, st, g2_slot(st));
    }
    BAR_VL(6);
    if (pub_ch >= 0) conv_publish(ws, tag, pub_ch, true, w & 3, lane);
#pragma unroll
    for (int st = 3; st < G2_R; ++st) { g2_issue_w(c, st, g2_slot(st)); g2_issue_h(c, st, g2_slot(st)); }
    BAR_VL(20);
    BAR_VL(10);
    g2_issue_w(c, 7, g2_slot(0)); g2_issue_h(c, 7, g2_slot(0)); g2_issue_w(c, 8, g2_slot(1)); g2_issue_h(c, 8, g2_slot(1));
#pragma unroll
    for (int k = 2; k < 11; ++k) {
      BAR_VL(10);
      g2_issue_w(c, 2 * k + 5, g2_slot(2 * k - 2)); g2_issue_h(c, 2 * k + 5, g2_slot(2 * k - 2));
      if (2 * k + 6 < 27) { g2_issue_w(c, 2 * k + 6, g2_slot(2 * k - 1)); g2_issue_h(c, 2 * k + 6, g2_slot(2 * k - 1)); }
    }
    {
      unsigned xo;
#pragma unroll
      for (int j = 0; j < 3; ++j) { const int pc = 3 * lw + j, row = 8 * pc + (lane >> 3); int r0 = 8 * pc; r0 = mt * 96 + r0 < L_ - 8 ? r0 : L_ - 8 - mt * 96;
        xo = (r0 + (lane >> 3)) * (D_ * 2) + (((lane & 7) ^ (row & 7)) << 4);
        glds16((const char*)(x + (size_t)mt * 96 * D_ + nu * 64), xo, l0 + LD_XT + pc * 1024); }
    }
    BAR_VL(13);
    {
      const char* rdy = ws + WS_CHRDY + (size_t)(nu * 64 * 4) * 8 + lane * 8;
      unsigned spins = 0;
      while (!__all(ld_agent(rdy) == tag && ld_agent(rdy + 512) == tag && ld_agent(rdy + 1024) == tag && ld_agent(rdy + 1536) == tag) && ++spins < SPIN_CAP) __builtin_amdgcn_s_sleep(2);
      TMO(ws, spins, 3);
      const char* t1 = ws + WS_T1 + (size_t)nu * 64 * (L_ * 2) + mt * 192;
#pragma unroll
      for (int j = 0; j < 3; ++j) { const int id = (3 * lw + j) * 64 + lane; glds16_dev(t1, (id / 12) * (L_ * 2) + (id % 12) * 16, l0 + (3 * lw + j) * 1024); }
    }
    AST(9);
    BAR_L();
    BAR_VL(0);
  } else {
    const int wm = w >> 1, wn = w & 1;
    unsigned xa[3][2], wa[3][2];
    {
      const int psi = (fr >> 1) & 7, q = fr >> 2, p = lane & 3, phi = ((q >> 1) & 1) + 2 * (g & 1);
#pragma unroll
      for (int sg = 0; sg < 3; ++sg)
#pragma unroll
        for (int k = 0; k < 2; ++k) {
          xa[sg][k] = l0 + sg * 3 * G2_SLOT + (48 * wm + fr) * 128 + (((4 * k + g) ^ psi) << 4); OPAQUE(xa[sg][k]);
          wa[sg][k] = l0 + sg * 3 * G2_SLOT + G2_W + (8 * g + q) * 128 + ((2 * ((2 * wn + k) ^ phi) + (p >> 1)) << 4) + 8 * (p & 1); OPAQUE(wa[sg][k]);
        }
    }
    f32x4 acc[2][3];
#pragma unroll
    for (int n = 0; n < 2; ++n)
#pragma unroll
      for (int m = 0; m < 3; ++m) acc[n][m] = (f32x4){0.f, 0.f, 0.f, 0.f};
    G2Frag F[2];
    auto rd1 = [&](G2Frag& f, int slot, int r) {
      const int ks = r / 7, q = r % 7, sg = slot / 3, so = (slot % 3) * G2_SLOT;
      if (q == 0) f.WL[ks][0] = trread(wa[sg][0] + so + ks * 4096); else if (q == 1) f.WH[ks][0] = trread(wa[sg][0] + so + ks * 4096 + 512);
      else if (q == 3) f.WL[ks][1] = trread(wa[sg][1] + so + ks * 4096); else if (q == 4) f.WH[ks][1] = trread(wa[sg][1] + so + ks * 4096 + 512);
      else { const int m = q == 2 ? 0 : q - 4; f.X[ks][m] = *(const LAS s16x8*)(size_t)(xa[sg][ks] + so + m * 2048); }
    };
    auto iter = [&](int set, bool rd, int slot) {
#pragma unroll
      for (int n = 0; n < 12; ++n) {
        const int ks = n / 6, m = (n % 6) >> 1, nb = n & 1;
        acc[nb][m] = mfma_bf16(cat8(F[set].WL[ks][nb], F[set].WH[ks][nb]), F[set].X[ks][m], acc[nb][m]);
        if (rd && n < 7) { rd1(F[set ^ 1], slot, 2 * n); rd1(F[set ^ 1], slot, 2 * n + 1); }
        SCHED();
      }
    };
    WAIT_V(0);
    if (pub_ch >= 0) conv_publish(ws, tag, pub_ch, true, w & 3, lane);
    BAR_L();
    AST(13);
#pragma unroll
    for (int r = 0; r < 14; ++r) rd1(F[0], 0, r);
    SCHED();
#pragma unroll
    for (int kt = 0; kt < 26; ++kt) {
      if ((kt & 1) == 0) { asm volatile("" ::: "memory"); __builtin_amdgcn_s_barrier(); asm volatile("" ::: "memory"); }
      SCHED(); iter(kt & 1, true, g2_slot(kt + 1));
    }
    AST(10);
    iter(0, false, 0);
    BAR_L();
    AST(11);
    s16x4 tq[2][3]; u32x2 xv[2][3]; unsigned xp[2][3];
#pragma unroll
    for (int n = 0; n < 2; ++n)
#pragma unroll
      for (int m = 0; m < 3; ++m) {
        const int s = 2 * wn + n, mb = 3 * wm + m;
        tq[n][m] = trread(l0 + (16 * s + 4 * g + (fr >> 2)) * 192 + (16 * mb + 4 * (lane & 3)) * 2);
        const int row = 16 * mb + fr, ch2 = 2 * s + (g >> 1);
        xp[n][m] = l0 + LD_XT + row * 128 + ((ch2 ^ (row & 7)) << 4) + 8 * (g & 1);
        xv[n][m] = *(const LAS u32x2*)(size_t)xp[n][m];
      }
    SCHED();
#pragma unroll
    for (int n = 0; n < 2; ++n)
#pragma unroll
      for (int m = 0; m < 3; ++m) {
        const f32x4 y = acc[n][m];
        const u32x2 tu = __builtin_bit_cast(u32x2, tq[n][m]);
        const unsigned x0 = xv[n][m][0], x1 = xv[n][m][1], t0 = tu[0], t1 = tu[1];
        *(LAS u32x2*)(size_t)xp[n][m] = (u32x2){pk_bf16((unpk_bf16(x0) + unpk_bf16(t0)) + (f32x2){y[0], y[1]}), pk_bf16((unpk_bf16(x1) + unpk_bf16(t1)) + (f32x2){y[2], y[3]})};
      }
  }
  BAR_L();
#pragma unroll
  for (int k = 0; k < 2; ++k) {
    const int pc = w + 8 * k;
    if (pc < 12) { const int row = 8 * pc + (lane >> 3), ch2 = lane & 7, tok = mt * 96 + row;
      const u32x4 v = *(const LAS u32x4*)(lds + LD_XT + row * 128 + ((ch2 ^ (row & 7)) << 4));
      if (tok < L_) *(u32x4*)(out + (size_t)tok * D_ + nu * 64 + ch2 * 8) = v; }
  }
}

__global__ __launch_bounds__(512, 2) void k_fused(const u16* __restrict__ x, const u16* __restrict__ w1, const u16* __restrict__ w3, const u16* __restrict__ w2, AuxIn in, char* ws, u16* __restrict__ out) {
  extern __shared__ __attribute__((aligned(16))) char shm[];
  ldsp lds = (ldsp)shm;
  const unsigned l0 = ldsa(lds);
  const int tid = threadIdx.x, w = __builtin_amdgcn_readfirstlane(tid >> 6), lane = tid & 63;
  const int b = blockIdx.x, xcd = b & 7, r = b >> 3, ng = xcd & 3, mg = xcd >> 2;
  const int nn = ng < 3 ? 7 : 6, gq = w >> 2, v = w & 3;
  {
    u64 pc; asm volatile("s_getpc_b64 %0" : "=s"(pc));
    const int ln = r * 14 + (lane < 14 ? lane : 13);
    if (w == 7) touch((const void*)(pc & ~127ull), (ln < KCODE_LINES ? ln : KCODE_LINES - 1) * 128, l0 + LD_SCR);
  }
  STDECL(st0); STDECL(st1); STDECL(st2); AMPDECL;
  AMP(1);
  STR(st0, 0);
  const bool gwg = r < 4 * nn;
  int mt2 = -1, nu2 = 0;
  if (r < (ng < 3 ? 28 : 26)) { const int ti = 28 * ng + r; mt2 = ti / 5; nu2 = (xcd >> 2) * 5 + ti % 5; }
  u64 tag;
  G2Ctx c2; bool h01 = false; int pub_ch = -1;
  if (gwg) {
    const int mt = mg * 4 + (r & 3), nu = ng * 7 + (r >> 2);
    const int gi = (xcd < 3 ? xcd * 28 : xcd == 3 ? 84 : xcd < 7 ? 108 + (xcd - 4) * 28 : 192) + r;
    const int ch = 2 * gi + gq;
    const bool chst = true;
    G1Ctx c; g1_setup(c, x, w1, w3, mt, nu, lds, w, lane);
    AuxPar par; aux_load(par, in, ch, v, lane);
    u64 nonce = ld_agent_async(ws + WS_NONCE);
    WAIT_V_TIE(0, nonce);
    AUX_WAIT(0, par);
    g1_issue(c, 0, 0); g1_issue(c, 1, 1);
    {
      const int xi = tid * nn + (r >> 2), xc = xi < 2048 ? xi : 2047;
      touch(c.xs, (xc >> 3) * (D_ * 2) + (2 + (xc & 7)) * 128, l0 + LD_SCR);
      const int wi = (tid & 255) * 4 + (r & 3);
      if (w < 4) touch(w < 2 ? c.wg : c.wu, (128 + (wi & 511)) * (F_ * 2), l0 + LD_SCR);
    }
    tag = nonce + 1;
    if (tid == 0) st_agent(ws + WS_ARR + b * 8, tag);
    const unsigned kc = l0 + LD_KC + gq * KC_BYTES, cv = l0 + LD_CV + gq * CV_U, ub = l0 + LD_U + gq * CV_UB, red = l0 + LD_RED + gq * CV_RED;
    AuxCh ach;
    kgen4(par, ach, l0 + LD_TAB + gq * KG_TAB, kc, v, lane STAMPPASS(st1), []() {});
    BAR_VL(0);
    STR(st0, 1); STC(st0, 8); AMP(2);
    g1_issue(c, 2, 2);
    f32x4 acc[4][4], cs; u64 st[4]; bool ut_ok = false;
    g1_main(acc, cs, st, ut_ok, [&](bool ok) {
      if (ok) conv_issue_u(ws, ch, ub, v, lane);
      const int cnt = nn == 7 ? 309 : 360, idr = r * cnt + (tid < cnt ? tid : cnt - 1), id = idr < 8640 ? idr : 8639;
      touch(w2 + (xcd >> 2) * 320, (id / 5) * (D_ * 2) + (id % 5) * 128, l0 + LD_SCR);
    }, c, lds, w, lane, ws, mt, tag);
    STR(st0, 2); STC(st0, 9); AMP(3);
    if (!ut_ok) {
      unsigned spins = 0; while (!__all(ld_agent(ws + WS_UTRDY + (lane & 31) * 8) == tag) && ++spins < SPIN_CAP) __builtin_amdgcn_s_sleep(2); TMO(ws, spins, 4);
      conv_issue_u(ws, ch, ub, v, lane);
    }
    LAS float* cst = (LAS float*)(lds + LD_CST);
    const int wm = w >> 1, wn = w & 1;
    if ((lane & 15) == 0) *(LAS f32x4*)(cst + (wn * 4 + wm) * 16 + 4 * (lane >> 4)) = cs;
    BAR_L();
    ldsp stg = lds + G1_SLOT;
    if (wm & 2) g1_epi_math<true>(acc, cst, st, stg, w, lane); else g1_epi_math<false>(acc, cst, st, stg, w, lane);
    BAR_L();
    STR(st0, 3); AMP(4);
    {
      const auto rs = __builtin_amdgcn_make_buffer_rsrc((void*)(ws + WS_H), 0, L_ * F_ * 2, 0x00020000);
      u32x4 hv[4];
#pragma unroll
      for (int k = 0; k < 4; ++k) { const int row = 32 * w + 8 * k + (lane >> 3), ch2 = lane & 7; hv[k] = *(const LAS u32x4*)(stg + row * 128 + ((ch2 ^ (row & 7)) << 4)); }
#pragma unroll
      for (int k = 0; k < 4; ++k) { const int row = 32 * w + 8 * k + (lane >> 3), ch2 = lane & 7;
        __builtin_amdgcn_raw_buffer_store_b128(hv[k], rs, ((mt * 256 + row) * F_ + nu * 64 + ch2 * 8) * 2, 0, 16); }
    }
    STR(st1, 4);
    conv_expand(kc, cv, ub, v, lane);
    STR(st1, 5);
    BAR_VL(0);
    if (tid == 0) st_agent(ws + WS_HRDY + mt * 256 + nu * 8, tag);
    STR(st0, 4); AMP(5);
    if (w >= 4) { g2_setup(c2, ws, w2, mt2, nu2, l0, w - 4, lane); g2_issue_w(c2, 0, g2_slot(0)); }
    conv_mm(cv, ub, red, v, lane);
    STR(st1, 6);
    u64 hf = ld_agent_async_v(ws, g2_rdy_off(mt2, lane));
    BAR_L();
    conv_finish(ach, ws, ch, chst, ub, red, v, lane);
    WAIT_V_TIE(2, hf);
    if (w >= 4) {
      g2_issue_w(c2, 1, g2_slot(1)); g2_issue_w(c2, 2, g2_slot(2));
      h01 = __all(hf == tag);
      if (h01) {
#pragma unroll
        for (int st = 0; st < 3; ++st) g2_issue_h(c2, st, g2_slot(st));
      }
    }
    STR(st1, 7);
    pub_ch = chst ? ch : -1;
    BAR_L();
    STR(st0, 5); AMP(6);
  } else {
    const int sid = ng < 3 ? ng * 4 + (r - 28) : 12 + (r - 24);
    if (r == 31) {
      if (w < 3) touch(ws, (tid < 188 ? tid : 188) << 16, l0 + LD_SCR);
      else if (w == 3) touch(out, (lane < 39 ? lane : 39) << 16, l0 + LD_SCR);
      else if (w == 4) touch(w2, (lane < 33 ? lane : 33) << 16, l0 + LD_SCR);
    }
    const int si = (xcd < 3 ? 4 * xcd : xcd == 3 ? 12 : xcd < 7 ? 20 + 4 * (xcd - 4) : 32) + (r - (ng < 3 ? 28 : 24));
    const int fi = si - (si > 13 ? 2 : 0) - (si > 33 ? 2 : 0);
    const int rounds = mt2 >= 0 ? 1 : (fi < 28 ? 3 : 2);
    const int ch0 = mt2 >= 0 ? 432 + 2 * ((si > 13 ? 2 : 0) + (si & 1)) : 440 + (fi < 28 ? 6 * fi : 168 + 4 * (fi - 28));
    AuxPar par; aux_load(par, in, (ch0 + gq < 640 ? ch0 + gq : 639), v, lane);
    if (sid < 16) tag = ln_block(x, ws, mg * 16 + sid, b, lds STAMPPASS(st2));
    else {
      tag = ld_agent(ws + WS_NONCE) + 1;
      if (tid == 0) st_agent(ws + WS_ARR + b * 8, tag);
      if (sid == 16 && mg == 0 && w == 0) {
        unsigned spins = 0;
        for (;;) {
          bool ok = true;
#pragma unroll
          for (int i = 0; i < 4; ++i) ok &= ld_agent(ws + WS_ARR + (lane + 64 * i) * 8) == tag;
          if (__all(ok) || ++spins >= SPIN_CAP) break;
          __builtin_amdgcn_s_sleep(8);
        }
        TMO(ws, spins, 5);
        if (lane == 0) st_agent(ws + WS_NONCE, tag);
      }
    }
    STR(st0, 6); AMP(2);
    BAR_L();
    const unsigned kc = l0 + LD_KC + gq * KC_BYTES, cv = l0 + LD_CV + gq * CV_U, ub = l0 + LD_U + gq * CV_UB, red = l0 + LD_RED + gq * CV_RED;
#pragma unroll 1
    for (int rd = 0; rd < rounds; ++rd) {
      const int chr = ch0 + 2 * rd + gq; const bool chst = chr < 640; const int ch = chst ? chr : 639;
      AUX_WAIT(0, par);
      if (pub_ch >= 0) { conv_publish(ws, tag, pub_ch, true, v, lane); pub_ch = -1; }
      if (rd > 0) conv_issue_u(ws, ch, ub, v, lane);
      if (rd == 0) STR(st1, 0);
      AuxCh ach;
      kgen4(par, ach, l0 + gq * KG_TAB, kc, v, lane STAMPPASS(st1), []() {});
      if (rd == 0) {
        unsigned spins = 0;
        while (!__all(ld_agent(ws + WS_UTRDY + (lane & 31) * 8) == tag) && ++spins < SPIN_CAP) __builtin_amdgcn_s_sleep(4);
        TMO(ws, spins, 6);
        STR(st0, 7); AMP(3);
        conv_issue_u(ws, ch, ub, v, lane);
      }
      if (rd + 1 < rounds) { const int cn = ch0 + 2 * (rd + 1) + gq; aux_load(par, in, cn < 640 ? cn : 639, v, lane); }
      BAR_L();
      STR(st1, 4);
      conv_expand(kc, cv, ub, v, lane);
      STR(st1, 5);
      if (rd + 1 < rounds) BAR_VL(6); else BAR_VL(0);
      conv_mm(cv, ub, red, v, lane);
      STR(st1, 6);
      BAR_L();
      conv_finish(ach, ws, ch, chst, ub, red, v, lane);
      STR(st1, 7);
      pub_ch = chst ? ch : -1;
      BAR_L();
      if (rd == 0) { STFLUSH(st1, ws, 3, 0); AMP(4); }
    }
    STR(st0, 5); AMP(6);
    if (mt2 >= 0 && w >= 4) {
      g2_setup(c2, ws, w2, mt2, nu2, l0, w - 4, lane);
#pragma unroll
      for (int st = 0; st < 3; ++st) g2_issue_w(c2, st, g2_slot(st));
    }
  }
  if (mt2 >= 0) gemm2_tile(x, w2, ws, out, tag, mt2, nu2, c2, h01, pub_ch, lds, w, lane STAMPPASS(st0));
  else { WAIT_V(0); if (pub_ch >= 0) conv_publish(ws, tag, pub_ch, true, v, lane); }
  STR(st0, 12); AMP(9);
  AMPEND();
  STFLUSH(st0, ws, gwg ? 0 : 5, 0); STFLUSH(st0, ws, 1, 4); if (gwg) { STFLUSH(st1, ws, 2, 0); } else { STFLUSH(st2, ws, 4, 0); }
}

extern "C" void kernel_launch(void* const* d_in, const int* in_sizes, int n_in, void* d_out, int out_size, void* d_ws, size_t ws_size, hipStream_t stream) {
  (void)hipFuncSetAttribute((const void*)k_fused, hipFuncAttributeMaxDynamicSharedMemorySize, LD_BYTES);
  AuxIn in{(const u16*)d_in[1], (const u16*)d_in[2], (const u16*)d_in[3], (const u16*)d_in[4], (const u16*)d_in[5], (const u16*)d_in[6]};
  k_fused<<<256, 512, LD_BYTES, stream>>>((const u16*)d_in[0], (const u16*)d_in[8], (const u16*)d_in[9], (const u16*)d_in[10], in, (char*)d_ws, (u16*)d_out);
}
```
